```python
import torch

def _softmax(x, axis=-1):
    return torch.softmax(x, dim=axis)
def _gelu(x):
    return torch.nn.functional.gelu(x, approximate="tanh")
def _silu(x):
    return torch.nn.functional.silu(x)
def _sigmoid(x):
    return torch.sigmoid(x)
def _layernorm(x, axis=-1, eps=1e-5):
    m = x.mean(dim=axis, keepdim=True)
    v = x.var(dim=axis, keepdim=True, unbiased=False)
    return (x - m) / torch.sqrt(v + eps)
def _scatter_add(tgt, idx, src, axis=0):
    return tgt.clone().index_add_(axis, idx, src)
def _segment_sum(src, seg, n):
    out = torch.zeros((n,) + src.shape[1:], dtype=src.dtype)
    return out.index_add_(0, seg, src)
def _pad(x, pw):
    flat = tuple(v for pair in reversed(pw) for v in pair)
    return torch.nn.functional.pad(x, flat)

kernel_name = 'gated_mix__softmax_full_mha__dense_activation_silu__prompt'

def setup_inputs():

    _w = (lambda a: (lambda t: t.to(torch.bfloat16) if t.is_floating_point() else t)(torch.as_tensor(a)))
    def _A_setup():
        B, S, D = 1, 2048, 512
        return {
            "x": _w((lambda *s: torch.randn(*s, dtype=torch.bfloat16) * 0.05)(B, S, D)),
            "wqkv": _w((lambda *s: torch.randn(*s, dtype=torch.bfloat16) * 0.8)(D, 3 * D)),
            "wo": _w((lambda *s: torch.randn(*s, dtype=torch.bfloat16) * 0.05)(D, D)),
        }
    _A_in = _A_setup()

    def _B_setup():
        B, S, D, F = 1, 2048, 512, 1792
        return {
            "x": _w((lambda *s: torch.randn(*s, dtype=torch.bfloat16) * 0.05)(B, S, D)),
            "w1": _w((lambda *s: torch.randn(*s, dtype=torch.bfloat16) * 3.2)(D, F) * (D ** -0.5)),
            "w2": _w((lambda *s: torch.randn(*s, dtype=torch.bfloat16) * 0.8)(F, D) * (F ** -0.5)),
        }
    _B_in = _B_setup()

    return {
        "A_x": _A_in["x"],
        "A_wqkv": _A_in["wqkv"],
        "A_wo": _A_in["wo"],
        "B_x": _B_in["x"],
        "B_w1": _B_in["w1"],
        "B_w2": _B_in["w2"],
        "gate_w": _w((lambda *s: torch.randn(*s, dtype=torch.bfloat16) * 0.05)(512) * 16.0),
    }

def reference(A_x, A_wqkv, A_wo, B_x, B_w1, B_w2, gate_w):
    def _A_ref(x, wqkv, wo):
        B, S, D = x.shape
        H = 32; Dh = D // H
        qkv = (lambda x, *d: x.permute(*d))((x @ wqkv).reshape(B, S, 3, H, Dh), 2, 0, 3, 1, 4)
        q, k, v = qkv[0], qkv[1], qkv[2]
        scale = Dh ** -0.5
        scores = torch.einsum("bhqd,bhkd->bhqk", q, k) * scale
        p = _softmax(scores, axis=-1)
        o = torch.einsum("bhqk,bhkd->bhqd", p, v)
        return (lambda x, *d: x.permute(*d))(o, 0, 2, 1, 3).reshape(B, S, D) @ wo

    def _B_ref(x, w1, w2):
        h = torch.einsum("bsd,df->bsf", x, w1)
        h = _silu(h)
        out = torch.einsum("bsf,fd->bsd", h, w2)
        return out

    x = A_x
    g = _sigmoid((x * gate_w).sum(-1, keepdims=True))
    a = _A_ref(x, A_wqkv, A_wo)
    b = _B_ref(x, B_w1, B_w2)
    return g * a + (1 - g) * b
```

```cpp
#include <hip/hip_runtime.h>
#include <hip/hip_bf16.h>
#include <stdint.h>

typedef __bf16 bf16x8 __attribute__((ext_vector_type(8)));
typedef __bf16 bf16x16 __attribute__((ext_vector_type(16)));
typedef __bf16 bf16x2 __attribute__((ext_vector_type(2)));
typedef float f32x16 __attribute__((ext_vector_type(16)));
typedef float f32x8 __attribute__((ext_vector_type(8)));
typedef float f32x4 __attribute__((ext_vector_type(4)));
typedef float f32x2 __attribute__((ext_vector_type(2)));
typedef unsigned u32x4 __attribute__((ext_vector_type(4)));
typedef unsigned u32x2 __attribute__((ext_vector_type(2)));
typedef short s16x4 __attribute__((ext_vector_type(4)));
typedef short s16x8 __attribute__((ext_vector_type(8)));
typedef unsigned short u16;
#define LAS __attribute__((address_space(3)))
#define MFMA(a, b, c) __builtin_amdgcn_mfma_f32_32x32x16_bf16(a, b, c, 0, 0, 0)

constexpr int S_ = 2048, D_ = 512, F_ = 1792;
constexpr size_t WS_QF = 0, WS_KF = 2u << 20, WS_VF = 4u << 20, WS_OF = 6u << 20, WS_HF = 8u << 20,
                 WS_W2F = 16u << 20, WS_WOF = 18u << 20, WS_BF = 19u << 20, WS_G = 23u << 20;
constexpr float LOG2E = 1.4426950408889634f;

__device__ __forceinline__ unsigned pk2(float lo, float hi) {
  bf16x2 v = {(__bf16)lo, (__bf16)hi};
  return __builtin_bit_cast(unsigned, v);
}
__device__ __forceinline__ u32x4 pk8(const f32x16& a, int o, float sc) {
  return (u32x4){pk2(a[o] * sc, a[o + 1] * sc), pk2(a[o + 2] * sc, a[o + 3] * sc),
                 pk2(a[o + 4] * sc, a[o + 5] * sc), pk2(a[o + 6] * sc, a[o + 7] * sc)};
}
__device__ __forceinline__ float silu_f(float v) {
  return v * __builtin_amdgcn_rcpf(1.f + __builtin_amdgcn_exp2f(-LOG2E * v));
}
__device__ __forceinline__ void glds16(const void* sbase, unsigned voff, unsigned lds) {
  asm volatile("s_mov_b32 m0, %2\n\ts_nop 0\n\tglobal_load_lds_dwordx4 %0, %1" ::"v"(voff), "s"(sbase), "s"(lds) : "memory");
}
__device__ __forceinline__ bf16x8 lds_b128(unsigned a) { return *(const LAS bf16x8*)(a); }
__device__ __forceinline__ s16x4 lds_tr(unsigned a) {
  return __builtin_amdgcn_ds_read_tr16_b64_v4i16((LAS s16x4*)(a));
}

constexpr int K1_PITCH = 448, K1_XB = 16384, K1_WB = 64 * K1_PITCH, K1_SLOT = K1_XB + K1_WB, K1_GW = 3 * K1_SLOT,
              K1_LDS = K1_GW + 1024;

template <bool FFN>
__device__ __forceinline__ void k1_body(const u16* __restrict__ x, const u16* __restrict__ w, const u16* __restrict__ gw,
                                        char* __restrict__ ws, int rb, int g, unsigned lds0) {
  constexpr int NB = FFN ? 7 : 6;
  constexpr int LDW = FFN ? F_ : 3 * D_;
  const int lane = threadIdx.x & 63, wid = __builtin_amdgcn_readfirstlane(threadIdx.x >> 6);
  const int l31 = lane & 31, hi = lane >> 5;
  const bool gate = !FFN && g == 0;
  unsigned xo[4], wo_[7];
#pragma unroll
  for (int i = 0; i < 4; i++) {
    const int row = 32 * wid + 8 * i + (lane >> 3);
    xo[i] = (unsigned)((rb * 128 + row) * 1024 + (((lane & 7) ^ ((row >> 1) & 7)) << 4));
  }
#pragma unroll
  for (int i = 0; i < 7; i++) {
    const int lin = (7 * wid + i) * 1024 + lane * 16, k = lin / K1_PITCH, cb = lin % K1_PITCH;
    if (FFN) wo_[i] = (unsigned)(k * (LDW * 2) + g * 448 + cb);
    else { const int sec = min(cb >> 7, 2); wo_[i] = (unsigned)(k * (LDW * 2) + sec * 1024 + g * 128 + (cb & 127)); }
  }
  auto issue = [&](int c, int slot) {
    const char* xs = (const char*)x + c * 128;
    const char* wsrc = (const char*)w + (size_t)c * 64 * LDW * 2;
    const unsigned sb = lds0 + slot * K1_SLOT;
#pragma unroll
    for (int i = 0; i < 4; i++) glds16(xs, xo[i], sb + (4 * wid + i) * 1024);
#pragma unroll
    for (int i = 0; i < 7; i++) glds16(wsrc, wo_[i], sb + K1_XB + (7 * wid + i) * 1024);
  };
  if (wid == 0) glds16(gw, lane * 16, lds0 + K1_GW);
  issue(0, 0); issue(1, 1); issue(2, 2);

  f32x16 acc[NB], accg = {};
#pragma unroll
  for (int b = 0; b < NB; b++) acc[b] = f32x16{};
  const int xrow = 32 * wid + l31;
  unsigned xa[4];
#pragma unroll
  for (int ks = 0; ks < 4; ks++) xa[ks] = lds0 + xrow * 128 + (((2 * ks + hi) ^ ((xrow >> 1) & 7)) << 4);
  const unsigned wa = lds0 + K1_XB + (8 * hi + ((lane & 15) >> 2)) * K1_PITCH + ((lane >> 4) & 1) * 32 + (lane & 3) * 8;
  bf16x8 xf[2], wf[2][NB];
  auto rd = [&](int slot, int ks, int set) {
    xf[set] = lds_b128(xa[ks] + slot * K1_SLOT);
#pragma unroll
    for (int b = 0; b < NB; b++) {
      const unsigned a = wa + slot * K1_SLOT + ks * 16 * K1_PITCH + b * 64;
      const s16x4 lo = lds_tr(a), hv = lds_tr(a + 4 * K1_PITCH);
      wf[set][b] = __builtin_bit_cast(bf16x8, (s16x8){lo[0], lo[1], lo[2], lo[3], hv[0], hv[1], hv[2], hv[3]});
    }
  };
  asm volatile("s_waitcnt vmcnt(22)\n\ts_barrier" ::: "memory");
  rd(0, 0, 0);
#pragma unroll
  for (int c = 0; c < 8; c++) {
#pragma unroll
    for (int ks = 0; ks < 4; ks++) {
      const int set = ks & 1;
      if (ks < 3) rd(c % 3, ks + 1, set ^ 1);
      else if (c < 7) {
        if (c < 6) asm volatile("s_waitcnt vmcnt(11) lgkmcnt(0)\n\ts_barrier" ::: "memory");
        else asm volatile("s_waitcnt vmcnt(0) lgkmcnt(0)\n\ts_barrier" ::: "memory");
        if (c + 3 < 8) issue(c + 3, c % 3);
        rd((c + 1) % 3, 0, set ^ 1);
      }
#pragma unroll
      for (int b = 0; b < NB; b++) {
        if (!FFN && b >= 4) acc[b] = MFMA(xf[set], wf[set][b], acc[b]);
        else acc[b] = MFMA(wf[set][b], xf[set], acc[b]);
      }
      if (gate) accg = MFMA(lds_b128(lds0 + K1_GW + (c * 64 + ks * 16 + hi * 8) * 2), xf[set], accg);
    }
  }
  const int tt = 4 * rb + wid;
  if (FFN) {
#pragma unroll
    for (int b = 0; b < 7; b++) {
#pragma unroll
      for (int r = 0; r < 16; r++) acc[b][r] = silu_f(acc[b][r]);
      u32x4* dst = (u32x4*)(ws + WS_HF) + (size_t)(tt * 112 + 14 * g + 2 * b) * 64 + lane;
      dst[0] = pk8(acc[b], 0, 1.f);
      dst[64] = pk8(acc[b], 8, 1.f);
    }
  } else {
    const int kslot = (l31 & 19) | ((l31 & 4) << 1) | ((l31 & 8) >> 1);
#pragma unroll
    for (int b = 0; b < 2; b++) {
      const int head = 4 * g + 2 * b;
      u32x4* q = (u32x4*)(ws + WS_QF) + (size_t)(head * 64 + tt) * 64 + lane;
      q[0] = pk8(acc[b], 0, 0.25f * LOG2E);
      q[4096] = pk8(acc[b], 8, 0.25f * LOG2E);
      u32x4* k = (u32x4*)(ws + WS_KF) + (size_t)(head * 64 + tt) * 64 + hi * 32 + kslot;
      k[0] = pk8(acc[2 + b], 0, 1.f);
      k[4096] = pk8(acc[2 + b], 8, 1.f);
      u32x4* v = (u32x4*)(ws + WS_VF) + (size_t)((head + (l31 >> 4)) * 64 + tt) * 64 + hi * 16 + (l31 & 15);
      v[0] = pk8(acc[4 + b], 0, 1.f);
      v[32] = pk8(acc[4 + b], 8, 1.f);
    }
    if (gate && hi == 0) ((float*)(ws + WS_G))[tt * 32 + l31] = __builtin_amdgcn_rcpf(1.f + __builtin_amdgcn_exp2f(-LOG2E * accg[0]));
  }
}

__global__ __launch_bounds__(256, 1) void k1_kernel(const u16* __restrict__ x, const u16* __restrict__ wqkv,
                                                    const u16* __restrict__ w1, const u16* __restrict__ w2,
                                                    const u16* __restrict__ wo, const u16* __restrict__ gw,
                                                    char* __restrict__ ws) {
  extern __shared__ __attribute__((aligned(16))) char smem[];
  const unsigned lds0 = (unsigned)(uintptr_t)smem;
  const int b = blockIdx.x, g = b & 7, i = b >> 3, rb = i >> 1;
  if (i & 1) k1_body<true>(x, w1, gw, ws, rb, g, lds0);
  else k1_body<false>(x, wqkv, gw, ws, rb, g, lds0);
  const int lane = threadIdx.x & 63, wv = b * 4 + (threadIdx.x >> 6);
  for (int u = wv; u < 2304; u += 1024) {
    const int s = u >> 4, nt = u & 15;
    const u16* src = (s < 112 ? w2 + (size_t)s * 16 * 512 : wo + (size_t)(s - 112) * 16 * 512) + (4 * (lane >> 5)) * 512 + nt * 32 + (lane & 31);
    u32x4 v;
#pragma unroll
    for (int j = 0; j < 4; j++) {
      const int r0 = ((2 * j) & 3) + 8 * (j >> 1);
      v[j] = (unsigned)src[r0 * 512] | ((unsigned)src[(r0 + 1) * 512] << 16);
    }
    u32x4* dst = s < 112 ? (u32x4*)(ws + WS_W2F) + (size_t)(nt * 112 + s) * 64 : (u32x4*)(ws + WS_WOF) + (size_t)(nt * 32 + s - 112) * 64;
    dst[lane] = v;
  }
}

constexpr int K2_K = 0, K2_V = 65536, K2_ONES = 131072, K2_LDS = K2_ONES + 4096;
#define SMFMAC(a, b, c, idx) __builtin_amdgcn_smfmac_f32_32x32x32_bf16(a, b, c, idx, 0, 0)
__device__ __forceinline__ bf16x16 lo8(const f32x16& s) {
  return __builtin_bit_cast(bf16x16, __builtin_shufflevector(s, s, 0, 1, 2, 3, 4, 5, 6, 7));
}
__device__ __forceinline__ bf16x16 hi8(const f32x16& s) {
  return __builtin_bit_cast(bf16x16, __builtin_shufflevector(s, s, 8, 9, 10, 11, 12, 13, 14, 15));
}
__device__ __forceinline__ float xmax(float m) {
  auto rr = __builtin_amdgcn_permlane32_swap(__float_as_uint(m), __float_as_uint(m), false, false);
  return __builtin_fmaxf(__uint_as_float(rr[0]), __uint_as_float(rr[1]));
}
__device__ __forceinline__ float max16(const f32x16& s) {
  float m = s[0];
#pragma unroll
  for (int r = 1; r < 16; r++) m = __builtin_fmaxf(m, s[r]);
  return m;
}

__global__ __launch_bounds__(512, 2) void k2_kernel(char* __restrict__ ws) {
  extern __shared__ __attribute__((aligned(16))) char smem[];
  const unsigned lds0 = (unsigned)(uintptr_t)smem;
  const int lane = threadIdx.x & 63, wid = __builtin_amdgcn_readfirstlane(threadIdx.x >> 6);
  const int l31 = lane & 31, hi = lane >> 5;
  const int b = blockIdx.x, xcd = b & 7, i = b >> 3, head = 4 * xcd + (i & 3), qt = (i >> 2) * 8 + wid;
  const char* kg = ws + WS_KF + (size_t)head * 65536;
  const char* vg = ws + WS_VF + (size_t)head * 65536;
  const bf16x8 qf = *((const bf16x8*)(ws + WS_QF) + (size_t)(head * 64 + qt) * 64 + lane);
  *(LAS u32x2*)(lds0 + K2_ONES + threadIdx.x * 8) = (u32x2){0x3F803F80u, 0x3F803F80u};
  asm volatile("s_waitcnt vmcnt(0)" ::: "memory");
#pragma unroll
  for (int j = 0; j < 8; j++) {
    glds16(kg + (8 * j + wid) * 1024, lane * 16, lds0 + K2_K + (8 * j + wid) * 1024);
    glds16(vg + (8 * j + wid) * 1024, lane * 16, lds0 + K2_V + (8 * j + wid) * 1024);
  }
  const int idx = 0xDDDD;
  const unsigned ka = lds0 + K2_K + lane * 16;
  unsigned va = lds0 + (l31 < 16 ? K2_V : K2_ONES) + hi * 256 + (l31 & 15) * 16;
  const unsigned vstep = l31 < 16 ? 4096u : 0u;
  asm volatile("s_waitcnt vmcnt(14) lgkmcnt(0)\n\ts_barrier" ::: "memory");
  f32x16 s0 = MFMA(lds_b128(ka), qf, f32x16{});
  const float m = xmax(max16(s0));
  f32x16 cm;
#pragma unroll
  for (int r = 0; r < 16; r++) cm[r] = -m;
  f32x16 o = {};
  for (int jc = 0; jc < 16; jc++) {
    if (jc == 2) asm volatile("s_waitcnt vmcnt(8)\n\ts_barrier" ::: "memory");
    if (jc == 8) asm volatile("s_waitcnt vmcnt(0)\n\ts_barrier" ::: "memory");
#pragma unroll
    for (int t = 0; t < 4; t++) {
      f32x16 s = MFMA(lds_b128(ka + jc * 4096 + t * 1024), qf, cm);
      const bf16x8 v0 = lds_b128(va + t * 1024), v1 = lds_b128(va + t * 1024 + 512);
#pragma unroll
      for (int r = 0; r < 16; r++) s[r] = __builtin_amdgcn_exp2f(s[r]);
      o = SMFMAC(v0, lo8(s), o, idx);
      o = SMFMAC(v1, hi8(s), o, idx);
    }
    va += vstep;
  }
  {
    float chk = o[8];
#pragma unroll
    for (int r = 0; r < 8; r++) chk += 0.f * o[r];
    const bool bad = !(chk <= 0x1p60f);
    if (__builtin_expect(__any(bad), 0)) {
      float mt = -INFINITY;
      for (int t = 0; t < 64; t++) mt = __builtin_fmaxf(mt, max16(MFMA(lds_b128(ka + t * 1024), qf, f32x16{})));
      mt = xmax(mt);
#pragma unroll
      for (int r = 0; r < 16; r++) cm[r] = -mt;
      o = f32x16{};
      unsigned vb = lds0 + (l31 < 16 ? K2_V : K2_ONES) + hi * 256 + (l31 & 15) * 16;
      for (int t = 0; t < 64; t++) {
        f32x16 s = MFMA(lds_b128(ka + t * 1024), qf, cm);
        const bf16x8 v0 = lds_b128(vb), v1 = lds_b128(vb + 512);
#pragma unroll
        for (int r = 0; r < 16; r++) s[r] = __builtin_amdgcn_exp2f(s[r]);
        o = SMFMAC(v0, lo8(s), o, idx);
        o = SMFMAC(v1, hi8(s), o, idx);
        vb += vstep >> 2;
      }
    }
  }
  *((u32x4*)(ws + WS_OF) + (size_t)(qt * 32 + head) * 64 + lane) = pk8(o, 0, __builtin_amdgcn_rcpf(o[8]));

  const int tb = 4 * xcd + (i >> 3), cb = i & 7;
  const bf16x8* hf = (const bf16x8*)(ws + WS_HF) + (size_t)(2 * tb * 112 + 14 * wid) * 64 + lane;
  const bf16x8* wf = (const bf16x8*)(ws + WS_W2F) + (size_t)(2 * cb * 112 + 14 * wid) * 64 + lane;
  f32x16 acc[4] = {};
#pragma unroll
  for (int s = 0; s < 14; s++) {
    const bf16x8 h0 = hf[s * 64], h1 = hf[(112 + s) * 64], w0 = wf[s * 64], w1 = wf[(112 + s) * 64];
    acc[0] = MFMA(w0, h0, acc[0]); acc[1] = MFMA(w1, h0, acc[1]);
    acc[2] = MFMA(w0, h1, acc[2]); acc[3] = MFMA(w1, h1, acc[3]);
  }
  __syncthreads();
#pragma unroll
  for (int bk = 0; bk < 4; bk++)
#pragma unroll
    for (int rq = 0; rq < 4; rq++)
      *(LAS f32x4*)(lds0 + ((wid * 16 + bk * 4 + rq) * 64 + lane) * 16) = (f32x4){acc[bk][4 * rq], acc[bk][4 * rq + 1], acc[bk][4 * rq + 2], acc[bk][4 * rq + 3]};
  __syncthreads();
#pragma unroll
  for (int e = 0; e < 2; e++) {
    const int q = wid * 2 + e, bk = q >> 2, rq = q & 3;
    f32x4 sum = {};
#pragma unroll
    for (int w = 0; w < 8; w++) sum += *(const LAS f32x4*)(lds0 + ((w * 16 + q) * 64 + lane) * 16);
    *((f32x4*)(ws + WS_BF) + (size_t)(((2 * tb + (bk >> 1)) * 16 + 2 * cb + (bk & 1)) * 4 + rq) * 64 + lane) = sum;
  }
}

__global__ __launch_bounds__(256) void k3_kernel(const char* __restrict__ ws, u16* __restrict__ out) {
  const int lane = threadIdx.x & 63, wid = threadIdx.x >> 6, l31 = lane & 31, hi = lane >> 5;
  const int b = blockIdx.x, xcd = b & 7, i = b >> 3, tb = 4 * xcd + (i >> 3), cb = i & 7;
  const int tt = 2 * tb + (wid >> 1), nt = 2 * cb + (wid & 1);
  const bf16x8* of = (const bf16x8*)(ws + WS_OF) + (size_t)tt * 32 * 64 + lane;
  const bf16x8* wf = (const bf16x8*)(ws + WS_WOF) + (size_t)nt * 32 * 64 + lane;
  const f32x4* bf = (const f32x4*)(ws + WS_BF) + (size_t)(tt * 16 + nt) * 4 * 64 + lane;
  const float g = ((const float*)(ws + WS_G))[tt * 32 + l31];
  f32x4 bv[4];
#pragma unroll
  for (int rq = 0; rq < 4; rq++) bv[rq] = bf[rq * 64];
  f32x16 acc = {};
#pragma unroll
  for (int s = 0; s < 32; s++) acc = MFMA(wf[s * 64], of[s * 64], acc);
  u16* dst = out + (size_t)(tt * 32 + l31) * 512 + nt * 32 + 4 * hi;
#pragma unroll
  for (int rq = 0; rq < 4; rq++) {
    float v[4];
#pragma unroll
    for (int j = 0; j < 4; j++) v[j] = g * acc[4 * rq + j] + (1.f - g) * bv[rq][j];
    *(u32x2*)(dst + 8 * rq) = (u32x2){pk2(v[0], v[1]), pk2(v[2], v[3])};
  }
}

extern "C" void kernel_launch(void* const* d_in, const int* in_sizes, int n_in, void* d_out, int out_size, void* d_ws,
                              size_t ws_size, hipStream_t stream) {
  const u16* x = (const u16*)d_in[0];
  const u16* wqkv = (const u16*)d_in[1];
  const u16* wo = (const u16*)d_in[2];
  const u16* w1 = (const u16*)d_in[4];
  const u16* w2 = (const u16*)d_in[5];
  const u16* gw = (const u16*)d_in[6];
  (void)hipFuncSetAttribute((const void*)k1_kernel, hipFuncAttributeMaxDynamicSharedMemorySize, K1_LDS);
  (void)hipFuncSetAttribute((const void*)k2_kernel, hipFuncAttributeMaxDynamicSharedMemorySize, K2_LDS);
  k1_kernel<<<256, 256, K1_LDS, stream>>>(x, wqkv, w1, w2, wo, gw, (char*)d_ws);
  k2_kernel<<<256, 512, K2_LDS, stream>>>((char*)d_ws);
  k3_kernel<<<256, 256, 0, stream>>>((const char*)d_ws, (u16*)d_out);
}
```

```cpp
#include <hip/hip_runtime.h>
#include <hip/hip_bf16.h>
#include <stdint.h>

typedef __bf16 bf16x8 __attribute__((ext_vector_type(8)));
typedef __bf16 bf16x16 __attribute__((ext_vector_type(16)));
typedef __bf16 bf16x2 __attribute__((ext_vector_type(2)));
typedef float f32x16 __attribute__((ext_vector_type(16)));
typedef float f32x8 __attribute__((ext_vector_type(8)));
typedef float f32x2 __attribute__((ext_vector_type(2)));
typedef unsigned u32x4 __attribute__((ext_vector_type(4)));
typedef unsigned u32x2 __attribute__((ext_vector_type(2)));
typedef unsigned short u16;
#define LAS __attribute__((address_space(3)))
#define GAS __attribute__((address_space(1)))
#define MFMA(a, b, c) __builtin_amdgcn_mfma_f32_32x32x16_bf16(a, b, c, 0, 0, 0)

constexpr int S_ = 2048, D_ = 512, F_ = 1792;
constexpr unsigned WS_QF = 0, WS_KF = 2u << 20, WS_VF = 4u << 20, WS_OF = 6u << 20, WS_HF = 8u << 20,
                 WS_G = 16u << 20, WS_MB = WS_G + 65536, WS_END = 17u << 20;
#define RB_OF(b) (((b) & 3) * 8 + (((b) >> 3) & 7))
#define CG_OF(b) ((((b) >> 2) & 1) * 4 + ((b) >> 6))
#define ROT_OF(b) (((b) & 3) * 2 + ((b) >> 6) * 2 + (((b) >> 2) & 1))
#define TW_OF(b) (((b) >> 3) & 7)
#define GROT_OF(b) (((b) & 3) * 2)
constexpr float LOG2E = 1.4426950408889634f;

__device__ __forceinline__ unsigned pk2(float lo, float hi) {
  bf16x2 v = {(__bf16)lo, (__bf16)hi};
  return __builtin_bit_cast(unsigned, v);
}
__device__ __forceinline__ u32x4 pk8(const f32x16& a, int o, float sc) {
  return (u32x4){pk2(a[o] * sc, a[o + 1] * sc), pk2(a[o + 2] * sc, a[o + 3] * sc),
                 pk2(a[o + 4] * sc, a[o + 5] * sc), pk2(a[o + 6] * sc, a[o + 7] * sc)};
}
__device__ __forceinline__ unsigned silu2(float a, float b) {
  const f32x2 v = {a, b}, t = v * -LOG2E;
  const f32x2 d = (f32x2){__builtin_amdgcn_exp2f(t[0]), __builtin_amdgcn_exp2f(t[1])} + 1.f;
  const f32x2 r = v * (f32x2){__builtin_amdgcn_rcpf(d[0]), __builtin_amdgcn_rcpf(d[1])};
  return pk2(r[0], r[1]);
}
constexpr unsigned OOR = 0x80000000u;
constexpr int SLOT = 49152;
constexpr int L_ONES = 3 * SLOT, L_Q = L_ONES + 4096, L_TOK = L_Q + 8192, LDS_BYTES = L_TOK + 512, L_PARK = 131072;
__device__ __forceinline__ u32x4 mkrs(const void* p, unsigned bytes) {
  const unsigned long long a = (unsigned long long)p;
  return (u32x4){(unsigned)a, (unsigned)(a >> 32), bytes, 0x00020000u};
}
#define WST(v, voff) __builtin_amdgcn_raw_buffer_store_b128(v, rs, voff, 0, 16)
#define WST1(v, voff) __builtin_amdgcn_raw_buffer_store_b32(v, rs, voff, 0, 16)
#define RFL(x) __builtin_amdgcn_readfirstlane((int)(x))
__device__ __forceinline__ int lane_now() {
  int l;
  asm volatile("v_mbcnt_lo_u32_b32 %0, -1, 0\n\tv_mbcnt_hi_u32_b32 %0, -1, %0" : "=v"(l));
  return l;
}
#define LANE() const int lane = lane_now(), l31 = lane & 31, hi = lane >> 5; const unsigned l16 = lane * 16; (void)l31; (void)hi; (void)l16
#define RS() char* ws_ = ws; asm volatile("" : "+s"(ws_)); const __amdgpu_buffer_rsrc_t rs = __builtin_amdgcn_make_buffer_rsrc(ws_, 0, (int)WS_END, 0x00020000)
#define BARRIER() do { __builtin_amdgcn_sched_barrier(0); asm volatile("s_barrier" ::: "memory"); __builtin_amdgcn_sched_barrier(0); } while (0)
#define SMFMAC(a, b, c, idx) __builtin_amdgcn_smfmac_f32_32x32x32_bf16(a, b, c, idx, 0, 0)
__device__ __forceinline__ bf16x16 lo8(const f32x16& s) {
  return __builtin_bit_cast(bf16x16, __builtin_shufflevector(s, s, 0, 1, 2, 3, 4, 5, 6, 7));
}
__device__ __forceinline__ bf16x16 hi8(const f32x16& s) {
  return __builtin_bit_cast(bf16x16, __builtin_shufflevector(s, s, 8, 9, 10, 11, 12, 13, 14, 15));
}
__device__ __forceinline__ float xmax(float m) {
  auto rr = __builtin_amdgcn_permlane32_swap(__float_as_uint(m), __float_as_uint(m), false, false);
  return __builtin_fmaxf(__uint_as_float(rr[0]), __uint_as_float(rr[1]));
}
__device__ __forceinline__ float max16(const f32x16& s) {
  float m = s[0];
#pragma unroll
  for (int r = 1; r < 16; r++) m = __builtin_fmaxf(m, s[r]);
  return m;
}

__device__ __forceinline__ bf16x8 bld(__amdgpu_buffer_rsrc_t rs, unsigned voff, unsigned soff) {
  return __builtin_bit_cast(bf16x8, __builtin_amdgcn_raw_buffer_load_b128(rs, voff, soff, 16));
}

__global__ __launch_bounds__(512, 2) void fused_kernel(const u16* x, const u16* wqkv,
                                                       const u16* wo, const u16* w1,
                                                       const u16* w2, const u16* gw,
                                                       char* ws, u16* out) {
  extern __shared__ __attribute__((aligned(16))) char smem[];
  unsigned long long pc;
  unsigned len;
  asm volatile("s_getpc_b64 %0\n.Lcode_from:\n\ts_mov_b32 %1, .Lcode_a-.Lcode_from\n\ts_max_u32 %1, %1, .Lcode_b-.Lcode_from\n\ts_max_u32 %1, %1, .Lcode_c-.Lcode_from"
               : "=s"(pc), "=s"(len)::"scc");
  const unsigned lds0 = (unsigned)(uintptr_t)smem;
  if (threadIdx.x < 64)
    asm volatile("s_mov_b32 m0, %2\n\ts_nop 0\n\tglobal_load_lds_dword %0, %1" ::"v"(min((((blockIdx.x >> 3) & 7) * 64 + threadIdx.x) * 128u, len)), "s"(pc),
                 "s"(lds0 + L_TOK + 256) : "memory");
  const int wid = __builtin_amdgcn_readfirstlane(threadIdx.x >> 6);
  const int b = blockIdx.x, rb = RB_OF(b), cg = CG_OF(b), head = 4 * cg + (rb & 3);
  const int kh = wid >> 2, kq = wid & 3;
  unsigned s_ko = WS_KF + head * 65536 + wid * 1024, s_ho = WS_HF + ((2 * rb + kh) * 112 + 28 * kq) * 1024;
  unsigned s_pw = ((wid >> 1) * 448 + (wid & 1) * 32) * 1024 + cg * 128, s_po = ((wid >> 1) * 128 + (wid & 1) * 16) * 1024 + cg * 128;
  unsigned s_ga = WS_G + (2 * rb + kh) * 128, s_os = WS_OF + ((2 * rb + kh) * 32 + 8 * kq) * 1024, s_l1 = wid * 1024;
  unsigned s_op = WS_OF + (((rb >> 2) * 8 + 2 * kq + kh) * 32 + head) * 1024, s_out = ((2 * rb + kh) * 32 * 512 + cg * 64 + kq * 16) * 2;
  unsigned s_gr = GROT_OF(b), s_tw = TW_OF(b) * 65536 + cg * 128;
  unsigned sm1 = WS_MB + cg * 256 + rb * 4, sm2 = WS_MB + (rb >> 1) * 4096 + (32 + (rb & 1) * 8 + cg) * 4;
  asm volatile("" : "+s"(sm1), "+s"(sm2), "+s"(s_gr), "+s"(s_tw), "+s"(s_ko), "+s"(s_ho), "+s"(s_pw), "+s"(s_po), "+s"(s_ga), "+s"(s_os), "+s"(s_l1), "+s"(s_op), "+s"(s_out));
  __builtin_amdgcn_sched_barrier(0);
  const int r2 = rb >> 1;
#define MSG_A(l) (WS_MB + (rb * 8 + cg) * 256 + (l) * 4)
#define INBOX(l) (((l) < 32 ? sm1 : sm2) + ((l) < 32 ? (l) * 2048 : ((l) & 15) * 256))
  unsigned msg_v = 0;
  u32x4 da = {OOR, OOR, OOR, OOR};
  f32x16 dd[2];
  const int qt0 = (rb >> 2) * 8 + 2 * kq;
  const int idx = 0xDDDD;
  unsigned ma = OOR;
  const bool odd = rb & 1;
  const int a0 = (448 * cg) & ~127, par = cg & 1;
  if (__builtin_expect(wid >= 4, 1)) {
    __builtin_amdgcn_s_setprio(3);
    LANE();
    const int r8 = lane >> 3, sw16 = ((lane & 7) ^ ((lane >> 2) & 4)) << 4;
    unsigned mw, tok = 0;
    u32x4 vo;
    u32x2 vo2;
    vo[0] = (r2 * 128 + 32 * kq + r8) * 1024 + (((lane & 7) ^ (r8 >> 1)) << 4);
    vo[1] = (vo[0] + 8192) ^ 64;
    vo[2] = (16 * kq + r8) * 3072 + cg * 128 + sw16 + (odd ? 0 : 1024);
    const unsigned vf = (16 * kq + r8) * 3584 + a0 + sw16;
    vo[3] = odd ? vf + 128 : vo[2] + 1024;
    unsigned hb = odd ? 8 * 3584 : 8 * 3072, ld = lds0 + kq * 4096, lw = lds0 + 16384 + kq * 2048;
    unsigned rot = ROT_OF(b);
    asm volatile("" : "+v"(vo), "+s"(hb), "+s"(ld), "+s"(lw), "+s"(rot));
    __builtin_amdgcn_sched_barrier(0);
    const u32x4 rx = mkrs(x, S_ * D_ * 2), rq = mkrs(wqkv, D_ * 3 * D_ * 2), rf = mkrs(w1, D_ * F_ * 2), rbb = odd ? rf : rq;
    unsigned t1, t2, cc;
#define L1IN "{v[0:3]}"(vo), [rx] "s"(rx), [ra] "s"(rq), [rb] "s"(rbb), [hb] "s"(hb), [ld] "s"(ld), [lq] "s"(lw), [rot] "s"(rot)
    asm volatile(
      "s_add_u32 %[cc], %[rot], 0\n\t"
      "s_and_b32 %[cc], %[cc], 7\n\t"
      "s_lshl_b32 %[t2], %[cc], 7\n\t"
      "s_add_u32 m0, %[ld], 0\n\t"
      "s_add_u32 %[t1], %[t2], 0\n\t"
      "buffer_load_dwordx4 v0, %[rx], %[t1] offen lds\n\t"
      "s_add_u32 m0, %[ld], 1024\n\t"
      "s_add_u32 %[t1], %[t2], 0\n\t"
      "buffer_load_dwordx4 v1, %[rx], %[t1] offen lds\n\t"
      "s_add_u32 m0, %[ld], 2048\n\t"
      "s_add_u32 %[t1], %[t2], 16384\n\t"
      "buffer_load_dwordx4 v0, %[rx], %[t1] offen lds\n\t"
      "s_add_u32 m0, %[ld], 3072\n\t"
      "s_add_u32 %[t1], %[t2], 16384\n\t"
      "buffer_load_dwordx4 v1, %[rx], %[t1] offen lds\n\t"
      "s_mul_i32 %[t2], %[cc], 196608\n\t"
      "s_add_u32 m0, %[lq], 0\n\t"
      "s_add_u32 %[t1], %[t2], 0\n\t"
      "buffer_load_dwordx4 v2, %[ra], %[t1] offen lds\n\t"
      "s_add_u32 m0, %[lq], 1024\n\t"
      "s_add_u32 %[t1], %[t2], 24576\n\t"
      "buffer_load_dwordx4 v2, %[ra], %[t1] offen lds\n\t"
      "s_lshl_b32 %[cc], %[cc], 3\n\t"
      "s_add_u32 m0, %[lq], 8192\n\t"
      "s_mul_i32 %[t1], %[hb], %[cc]\n\t"
      "buffer_load_dwordx4 v3, %[rb], %[t1] offen lds\n\t"
      "s_add_u32 m0, %[lq], 9216\n\t"
      "s_add_u32 %[t1], %[t1], %[hb]\n\t"
      "buffer_load_dwordx4 v3, %[rb], %[t1] offen lds\n\t"
        : [t1] "=&s"(t1), [t2] "=&s"(t2), [cc] "=&s"(cc) : L1IN : "memory", "scc");
    __builtin_amdgcn_sched_barrier(0);
    RS();
    const int ln2 = lane_now();
    const unsigned inbox = INBOX(ln2), msg_a = MSG_A(ln2);
    unsigned dm;
    asm volatile(
      "s_add_u32 %[cc], %[rot], 1\n\t"
      "s_and_b32 %[cc], %[cc], 7\n\t"
      "s_lshl_b32 %[t2], %[cc], 7\n\t"
      "s_add_u32 m0, %[ld], 32768\n\t"
      "s_add_u32 %[t1], %[t2], 0\n\t"
      "buffer_load_dwordx4 v0, %[rx], %[t1] offen lds\n\t"
      "s_add_u32 m0, %[ld], 33792\n\t"
      "s_add_u32 %[t1], %[t2], 0\n\t"
      "buffer_load_dwordx4 v1, %[rx], %[t1] offen lds\n\t"
      "s_add_u32 m0, %[ld], 34816\n\t"
      "s_add_u32 %[t1], %[t2], 16384\n\t"
      "buffer_load_dwordx4 v0, %[rx], %[t1] offen lds\n\t"
      "s_add_u32 m0, %[ld], 35840\n\t"
      "s_add_u32 %[t1], %[t2], 16384\n\t"
      "buffer_load_dwordx4 v1, %[rx], %[t1] offen lds\n\t"
      "s_mul_i32 %[t2], %[cc], 196608\n\t"
      "s_add_u32 m0, %[lq], 32768\n\t"
      "s_add_u32 %[t1], %[t2], 0\n\t"
      "buffer_load_dwordx4 v2, %[ra], %[t1] offen lds\n\t"
      "s_add_u32 m0, %[lq], 33792\n\t"
      "s_add_u32 %[t1], %[t2], 24576\n\t"
      "buffer_load_dwordx4 v2, %[ra], %[t1] offen lds\n\t"
      "s_lshl_b32 %[cc], %[cc], 3\n\t"
      "s_add_u32 m0, %[lq], 40960\n\t"
      "s_mul_i32 %[t1], %[hb], %[cc]\n\t"
      "buffer_load_dwordx4 v3, %[rb], %[t1] offen lds\n\t"
      "s_add_u32 m0, %[lq], 41984\n\t"
      "s_add_u32 %[t1], %[t1], %[hb]\n\t"
      "buffer_load_dwordx4 v3, %[rb], %[t1] offen lds\n\t"
      "s_load_dword %[dm], %[m1], 0\n\t"
      "s_load_dword %[dm], %[m1], 2048\n\t"
      "s_load_dword %[dm], %[m1], 4096\n\t"
      "s_load_dword %[dm], %[m1], 6144\n\t"
      "s_load_dword %[dm], %[m1], 8192\n\t"
      "s_load_dword %[dm], %[m1], 10240\n\t"
      "s_load_dword %[dm], %[m1], 12288\n\t"
      "s_load_dword %[dm], %[m1], 14336\n\t"
      "s_load_dword %[dm], %[m2], 0\n\t"
      "s_load_dword %[dm], %[m2], 256\n\t"
      "s_load_dword %[dm], %[m2], 512\n\t"
      "s_load_dword %[dm], %[m2], 768\n\t"
      "s_waitcnt vmcnt(8)\n\t"
      "s_barrier\n\t"
      "s_add_u32 %[cc], %[rot], 2\n\t"
      "s_and_b32 %[cc], %[cc], 7\n\t"
      "s_lshl_b32 %[t2], %[cc], 7\n\t"
      "s_add_u32 m0, %[ld], 65536\n\t"
      "s_add_u32 %[t1], %[t2], 0\n\t"
      "buffer_load_dwordx4 v0, %[rx], %[t1] offen lds\n\t"
      "s_add_u32 m0, %[ld], 66560\n\t"
      "s_add_u32 %[t1], %[t2], 0\n\t"
      "buffer_load_dwordx4 v1, %[rx], %[t1] offen lds\n\t"
      "s_add_u32 m0, %[ld], 67584\n\t"
      "s_add_u32 %[t1], %[t2], 16384\n\t"
      "buffer_load_dwordx4 v0, %[rx], %[t1] offen lds\n\t"
      "s_add_u32 m0, %[ld], 68608\n\t"
      "s_add_u32 %[t1], %[t2], 16384\n\t"
      "buffer_load_dwordx4 v1, %[rx], %[t1] offen lds\n\t"
      "s_mul_i32 %[t2], %[cc], 196608\n\t"
      "s_add_u32 m0, %[lq], 65536\n\t"
      "s_add_u32 %[t1], %[t2], 0\n\t"
      "buffer_load_dwordx4 v2, %[ra], %[t1] offen lds\n\t"
      "s_add_u32 m0, %[lq], 66560\n\t"
      "s_add_u32 %[t1], %[t2], 24576\n\t"
      "buffer_load_dwordx4 v2, %[ra], %[t1] offen lds\n\t"
      "s_lshl_b32 %[cc], %[cc], 3\n\t"
      "s_add_u32 m0, %[lq], 73728\n\t"
      "s_mul_i32 %[t1], %[hb], %[cc]\n\t"
      "buffer_load_dwordx4 v3, %[rb], %[t1] offen lds\n\t"
      "s_add_u32 m0, %[lq], 74752\n\t"
      "s_add_u32 %[t1], %[t1], %[hb]\n\t"
      "buffer_load_dwordx4 v3, %[rb], %[t1] offen lds\n\t"
      "s_waitcnt vmcnt(8)\n\t"
      "s_barrier\n\t"
      "buffer_load_dword %[mw], %[pa], %[rs], 0 offen sc1\n\t"
      "s_add_u32 %[cc], %[rot], 3\n\t"
      "s_and_b32 %[cc], %[cc], 7\n\t"
      "s_lshl_b32 %[t2], %[cc], 7\n\t"
      "s_add_u32 m0, %[ld], 98304\n\t"
      "s_add_u32 %[t1], %[t2], 0\n\t"
      "buffer_load_dwordx4 v0, %[rx], %[t1] offen lds\n\t"
      "s_add_u32 m0, %[ld], 99328\n\t"
      "s_add_u32 %[t1], %[t2], 0\n\t"
      "buffer_load_dwordx4 v1, %[rx], %[t1] offen lds\n\t"
      "s_add_u32 m0, %[ld], 100352\n\t"
      "s_add_u32 %[t1], %[t2], 16384\n\t"
      "buffer_load_dwordx4 v0, %[rx], %[t1] offen lds\n\t"
      "s_add_u32 m0, %[ld], 101376\n\t"
      "s_add_u32 %[t1], %[t2], 16384\n\t"
      "buffer_load_dwordx4 v1, %[rx], %[t1] offen lds\n\t"
      "s_mul_i32 %[t2], %[cc], 196608\n\t"
      "s_add_u32 m0, %[lq], 98304\n\t"
      "s_add_u32 %[t1], %[t2], 0\n\t"
      "buffer_load_dwordx4 v2, %[ra], %[t1] offen lds\n\t"
      "s_add_u32 m0, %[lq], 99328\n\t"
      "s_add_u32 %[t1], %[t2], 24576\n\t"
      "buffer_load_dwordx4 v2, %[ra], %[t1] offen lds\n\t"
      "s_lshl_b32 %[cc], %[cc], 3\n\t"
      "s_add_u32 m0, %[lq], 106496\n\t"
      "s_mul_i32 %[t1], %[hb], %[cc]\n\t"
      "buffer_load_dwordx4 v3, %[rb], %[t1] offen lds\n\t"
      "s_add_u32 m0, %[lq], 107520\n\t"
      "s_add_u32 %[t1], %[t1], %[hb]\n\t"
      "buffer_load_dwordx4 v3, %[rb], %[t1] offen lds\n\t"
      "s_add_u32 %[cc], %[rot], 4\n\t"
      "s_and_b32 %[cc], %[cc], 7\n\t"
      "s_lshl_b32 %[t2], %[cc], 7\n\t"
      "s_add_u32 m0, %[ld], 0\n\t"
      "s_add_u32 %[t1], %[t2], 0\n\t"
      "buffer_load_dwordx4 v0, %[rx], %[t1] offen lds\n\t"
      "s_add_u32 m0, %[ld], 1024\n\t"
      "s_add_u32 %[t1], %[t2], 0\n\t"
      "buffer_load_dwordx4 v1, %[rx], %[t1] offen lds\n\t"
      "s_add_u32 m0, %[ld], 2048\n\t"
      "s_add_u32 %[t1], %[t2], 16384\n\t"
      "buffer_load_dwordx4 v0, %[rx], %[t1] offen lds\n\t"
      "s_add_u32 m0, %[ld], 3072\n\t"
      "s_add_u32 %[t1], %[t2], 16384\n\t"
      "buffer_load_dwordx4 v1, %[rx], %[t1] offen lds\n\t"
      "s_mul_i32 %[t2], %[cc], 196608\n\t"
      "s_add_u32 m0, %[lq], 0\n\t"
      "s_add_u32 %[t1], %[t2], 0\n\t"
      "buffer_load_dwordx4 v2, %[ra], %[t1] offen lds\n\t"
      "s_add_u32 m0, %[lq], 1024\n\t"
      "s_add_u32 %[t1], %[t2], 24576\n\t"
      "buffer_load_dwordx4 v2, %[ra], %[t1] offen lds\n\t"
      "s_lshl_b32 %[cc], %[cc], 3\n\t"
      "s_add_u32 m0, %[lq], 8192\n\t"
      "s_mul_i32 %[t1], %[hb], %[cc]\n\t"
      "buffer_load_dwordx4 v3, %[rb], %[t1] offen lds\n\t"
      "s_add_u32 m0, %[lq], 9216\n\t"
      "s_add_u32 %[t1], %[t1], %[hb]\n\t"
      "buffer_load_dwordx4 v3, %[rb], %[t1] offen lds\n\t"
      "s_waitcnt vmcnt(16) lgkmcnt(0)\n\t"
      "s_barrier\n\t"
        : [t1] "=&s"(t1), [t2] "=&s"(t2), [cc] "=&s"(cc), [mw] "=&v"(mw), [dm] "=&s"(dm)
        : L1IN, [m1] "s"(ws + sm1 + kq * 16384), [m2] "s"(ws + sm2 + kq * 1024), [rs] "s"(rs), [pa] "v"(wid == 7 ? inbox : OOR)
        : "memory", "scc");
    {
      const int hit = __builtin_amdgcn_ds_permute((mw & 63) * 4, 1);
      tok = __builtin_ctzll(__ballot(hit == 0));
      if (wid == 7) *(LAS unsigned*)(lds0 + L_TOK) = tok;
      ma = wid == 7 ? msg_a : OOR;
      WST1(((mw >> 8) & 63) | (tok << 8), ma);
      asm volatile("s_waitcnt lgkmcnt(0)" ::: "memory");
    }
    asm volatile(
      "s_add_u32 %[cc], %[rot], 5\n\t"
      "s_and_b32 %[cc], %[cc], 7\n\t"
      "s_lshl_b32 %[t2], %[cc], 7\n\t"
      "s_add_u32 m0, %[ld], 32768\n\t"
      "s_add_u32 %[t1], %[t2], 0\n\t"
      "buffer_load_dwordx4 v0, %[rx], %[t1] offen lds\n\t"
      "s_add_u32 m0, %[ld], 33792\n\t"
      "s_add_u32 %[t1], %[t2], 0\n\t"
      "buffer_load_dwordx4 v1, %[rx], %[t1] offen lds\n\t"
      "s_add_u32 m0, %[ld], 34816\n\t"
      "s_add_u32 %[t1], %[t2], 16384\n\t"
      "buffer_load_dwordx4 v0, %[rx], %[t1] offen lds\n\t"
      "s_add_u32 m0, %[ld], 35840\n\t"
      "s_add_u32 %[t1], %[t2], 16384\n\t"
      "buffer_load_dwordx4 v1, %[rx], %[t1] offen lds\n\t"
      "s_mul_i32 %[t2], %[cc], 196608\n\t"
      "s_add_u32 m0, %[lq], 32768\n\t"
      "s_add_u32 %[t1], %[t2], 0\n\t"
      "buffer_load_dwordx4 v2, %[ra], %[t1] offen lds\n\t"
      "s_add_u32 m0, %[lq], 33792\n\t"
      "s_add_u32 %[t1], %[t2], 24576\n\t"
      "buffer_load_dwordx4 v2, %[ra], %[t1] offen lds\n\t"
      "s_lshl_b32 %[cc], %[cc], 3\n\t"
      "s_add_u32 m0, %[lq], 40960\n\t"
      "s_mul_i32 %[t1], %[hb], %[cc]\n\t"
      "buffer_load_dwordx4 v3, %[rb], %[t1] offen lds\n\t"
      "s_add_u32 m0, %[lq], 41984\n\t"
      "s_add_u32 %[t1], %[t1], %[hb]\n\t"
      "buffer_load_dwordx4 v3, %[rb], %[t1] offen lds\n\t"
      "s_waitcnt vmcnt(17)\n\t"
      "s_barrier\n\t"
      "s_add_u32 %[cc], %[rot], 6\n\t"
      "s_and_b32 %[cc], %[cc], 7\n\t"
      "s_lshl_b32 %[t2], %[cc], 7\n\t"
      "s_add_u32 m0, %[ld], 65536\n\t"
      "s_add_u32 %[t1], %[t2], 0\n\t"
      "buffer_load_dwordx4 v0, %[rx], %[t1] offen lds\n\t"
      "s_add_u32 m0, %[ld], 66560\n\t"
      "s_add_u32 %[t1], %[t2], 0\n\t"
      "buffer_load_dwordx4 v1, %[rx], %[t1] offen lds\n\t"
      "s_add_u32 m0, %[ld], 67584\n\t"
      "s_add_u32 %[t1], %[t2], 16384\n\t"
      "buffer_load_dwordx4 v0, %[rx], %[t1] offen lds\n\t"
      "s_add_u32 m0, %[ld], 68608\n\t"
      "s_add_u32 %[t1], %[t2], 16384\n\t"
      "buffer_load_dwordx4 v1, %[rx], %[t1] offen lds\n\t"
      "s_mul_i32 %[t2], %[cc], 196608\n\t"
      "s_add_u32 m0, %[lq], 65536\n\t"
      "s_add_u32 %[t1], %[t2], 0\n\t"
      "buffer_load_dwordx4 v2, %[ra], %[t1] offen lds\n\t"
      "s_add_u32 m0, %[lq], 66560\n\t"
      "s_add_u32 %[t1], %[t2], 24576\n\t"
      "buffer_load_dwordx4 v2, %[ra], %[t1] offen lds\n\t"
      "s_lshl_b32 %[cc], %[cc], 3\n\t"
      "s_add_u32 m0, %[lq], 73728\n\t"
      "s_mul_i32 %[t1], %[hb], %[cc]\n\t"
      "buffer_load_dwordx4 v3, %[rb], %[t1] offen lds\n\t"
      "s_add_u32 m0, %[lq], 74752\n\t"
      "s_add_u32 %[t1], %[t1], %[hb]\n\t"
      "buffer_load_dwordx4 v3, %[rb], %[t1] offen lds\n\t"
      "s_waitcnt vmcnt(17)\n\t"
      "s_barrier\n\t"
      "s_add_u32 %[cc], %[rot], 7\n\t"
      "s_and_b32 %[cc], %[cc], 7\n\t"
      "s_lshl_b32 %[t2], %[cc], 7\n\t"
      "s_add_u32 m0, %[ld], 98304\n\t"
      "s_add_u32 %[t1], %[t2], 0\n\t"
      "buffer_load_dwordx4 v0, %[rx], %[t1] offen lds\n\t"
      "s_add_u32 m0, %[ld], 99328\n\t"
      "s_add_u32 %[t1], %[t2], 0\n\t"
      "buffer_load_dwordx4 v1, %[rx], %[t1] offen lds\n\t"
      "s_add_u32 m0, %[ld], 100352\n\t"
      "s_add_u32 %[t1], %[t2], 16384\n\t"
      "buffer_load_dwordx4 v0, %[rx], %[t1] offen lds\n\t"
      "s_add_u32 m0, %[ld], 101376\n\t"
      "s_add_u32 %[t1], %[t2], 16384\n\t"
      "buffer_load_dwordx4 v1, %[rx], %[t1] offen lds\n\t"
      "s_mul_i32 %[t2], %[cc], 196608\n\t"
      "s_add_u32 m0, %[lq], 98304\n\t"
      "s_add_u32 %[t1], %[t2], 0\n\t"
      "buffer_load_dwordx4 v2, %[ra], %[t1] offen lds\n\t"
      "s_add_u32 m0, %[lq], 99328\n\t"
      "s_add_u32 %[t1], %[t2], 24576\n\t"
      "buffer_load_dwordx4 v2, %[ra], %[t1] offen lds\n\t"
      "s_lshl_b32 %[cc], %[cc], 3\n\t"
      "s_add_u32 m0, %[lq], 106496\n\t"
      "s_mul_i32 %[t1], %[hb], %[cc]\n\t"
      "buffer_load_dwordx4 v3, %[rb], %[t1] offen lds\n\t"
      "s_add_u32 m0, %[lq], 107520\n\t"
      "s_add_u32 %[t1], %[t1], %[hb]\n\t"
      "buffer_load_dwordx4 v3, %[rb], %[t1] offen lds\n\t"
      "s_waitcnt vmcnt(16)\n\t"
      "s_barrier\n\t"
        : [t1] "=&s"(t1), [t2] "=&s"(t2), [cc] "=&s"(cc) : L1IN : "memory", "scc");
    vo2[0] = odd ? vf + 256 : vf + par * 384;
    vo2[1] = (16 * kq + (ln2 >> 2)) * 3584 + a0 + (par ? 64 : 384) + (ln2 & 3) * 16;
#define L2OPS                                                                                                     \
  : [t1] "=&s"(t1), [t2] "=&s"(t2), [cc] "=&s"(cc), [mw] "=&v"(mw)                                                \
  : "{v[4:5]}"(vo2), [rb] "s"(rf), [ld0] "s"(lds0 + kq * 2048), [rot] "s"(rot), [rs] "s"(rs), [pa] "v"(wid == 7 ? inbox : OOR)    \
  : "memory", "scc"
    if (odd) asm volatile(
      "s_add_u32 %[cc], %[rot], 0\n\t"
      "s_and_b32 %[cc], %[cc], 7\n\t"
      "s_mul_i32 %[t2], %[cc], 229376\n\t"
      "s_add_u32 m0, %[ld0], 0\n\t"
      "s_add_u32 %[t1], %[t2], 0\n\t"
      "buffer_load_dwordx4 v4, %[rb], %[t1] offen lds\n\t"
      "s_add_u32 m0, %[ld0], 1024\n\t"
      "s_add_u32 %[t1], %[t2], 28672\n\t"
      "buffer_load_dwordx4 v4, %[rb], %[t1] offen lds\n\t"
      "s_add_u32 %[cc], %[rot], 1\n\t"
      "s_and_b32 %[cc], %[cc], 7\n\t"
      "s_mul_i32 %[t2], %[cc], 229376\n\t"
      "s_add_u32 m0, %[ld0], 8192\n\t"
      "s_add_u32 %[t1], %[t2], 0\n\t"
      "buffer_load_dwordx4 v4, %[rb], %[t1] offen lds\n\t"
      "s_add_u32 m0, %[ld0], 9216\n\t"
      "s_add_u32 %[t1], %[t2], 28672\n\t"
      "buffer_load_dwordx4 v4, %[rb], %[t1] offen lds\n\t"
      "s_add_u32 %[cc], %[rot], 2\n\t"
      "s_and_b32 %[cc], %[cc], 7\n\t"
      "s_mul_i32 %[t2], %[cc], 229376\n\t"
      "s_add_u32 m0, %[ld0], 16384\n\t"
      "s_add_u32 %[t1], %[t2], 0\n\t"
      "buffer_load_dwordx4 v4, %[rb], %[t1] offen lds\n\t"
      "s_add_u32 m0, %[ld0], 17408\n\t"
      "s_add_u32 %[t1], %[t2], 28672\n\t"
      "buffer_load_dwordx4 v4, %[rb], %[t1] offen lds\n\t"
      "s_add_u32 %[cc], %[rot], 3\n\t"
      "s_and_b32 %[cc], %[cc], 7\n\t"
      "s_mul_i32 %[t2], %[cc], 229376\n\t"
      "s_add_u32 m0, %[ld0], 24576\n\t"
      "s_add_u32 %[t1], %[t2], 0\n\t"
      "buffer_load_dwordx4 v4, %[rb], %[t1] offen lds\n\t"
      "s_add_u32 m0, %[ld0], 25600\n\t"
      "s_add_u32 %[t1], %[t2], 28672\n\t"
      "buffer_load_dwordx4 v4, %[rb], %[t1] offen lds\n\t"
      "s_waitcnt vmcnt(16)\n\t"
      "s_barrier\n\t"
      "s_add_u32 %[cc], %[rot], 4\n\t"
      "s_and_b32 %[cc], %[cc], 7\n\t"
      "s_mul_i32 %[t2], %[cc], 229376\n\t"
      "s_add_u32 m0, %[ld0], 32768\n\t"
      "s_add_u32 %[t1], %[t2], 0\n\t"
      "buffer_load_dwordx4 v4, %[rb], %[t1] offen lds\n\t"
      "s_add_u32 m0, %[ld0], 33792\n\t"
      "s_add_u32 %[t1], %[t2], 28672\n\t"
      "buffer_load_dwordx4 v4, %[rb], %[t1] offen lds\n\t"
      "s_add_u32 %[cc], %[rot], 5\n\t"
      "s_and_b32 %[cc], %[cc], 7\n\t"
      "s_mul_i32 %[t2], %[cc], 229376\n\t"
      "s_add_u32 m0, %[ld0], 40960\n\t"
      "s_add_u32 %[t1], %[t2], 0\n\t"
      "buffer_load_dwordx4 v4, %[rb], %[t1] offen lds\n\t"
      "s_add_u32 m0, %[ld0], 41984\n\t"
      "s_add_u32 %[t1], %[t2], 28672\n\t"
      "buffer_load_dwordx4 v4, %[rb], %[t1] offen lds\n\t"
      "s_add_u32 %[cc], %[rot], 6\n\t"
      "s_and_b32 %[cc], %[cc], 7\n\t"
      "s_mul_i32 %[t2], %[cc], 229376\n\t"
      "s_add_u32 m0, %[ld0], 49152\n\t"
      "s_add_u32 %[t1], %[t2], 0\n\t"
      "buffer_load_dwordx4 v4, %[rb], %[t1] offen lds\n\t"
      "s_add_u32 m0, %[ld0], 50176\n\t"
      "s_add_u32 %[t1], %[t2], 28672\n\t"
      "buffer_load_dwordx4 v4, %[rb], %[t1] offen lds\n\t"
      "s_add_u32 %[cc], %[rot], 7\n\t"
      "s_and_b32 %[cc], %[cc], 7\n\t"
      "s_mul_i32 %[t2], %[cc], 229376\n\t"
      "s_add_u32 m0, %[ld0], 57344\n\t"
      "s_add_u32 %[t1], %[t2], 0\n\t"
      "buffer_load_dwordx4 v4, %[rb], %[t1] offen lds\n\t"
      "s_add_u32 m0, %[ld0], 58368\n\t"
      "s_add_u32 %[t1], %[t2], 28672\n\t"
      "buffer_load_dwordx4 v4, %[rb], %[t1] offen lds\n\t"
      "s_waitcnt vmcnt(16)\n\t"
      "s_barrier\n\t"
      "s_waitcnt vmcnt(8)\n\t"
      "s_barrier\n\t"
      "buffer_load_dword %[mw], %[pa], %[rs], 0 offen sc1\n\t"
      "s_waitcnt vmcnt(1)\n\t"
      "s_barrier\n\t"
      "s_waitcnt vmcnt(0)\n\t"
          L2OPS);
    else asm volatile(
      "s_add_u32 %[cc], %[rot], 0\n\t"
      "s_and_b32 %[cc], %[cc], 7\n\t"
      "s_mul_i32 %[t2], %[cc], 229376\n\t"
      "s_add_u32 m0, %[ld0], 0\n\t"
      "s_add_u32 %[t1], %[t2], 0\n\t"
      "buffer_load_dwordx4 v4, %[rb], %[t1] offen lds\n\t"
      "s_add_u32 m0, %[ld0], 1024\n\t"
      "s_add_u32 %[t1], %[t2], 28672\n\t"
      "buffer_load_dwordx4 v4, %[rb], %[t1] offen lds\n\t"
      "s_add_u32 %[cc], %[rot], 1\n\t"
      "s_and_b32 %[cc], %[cc], 7\n\t"
      "s_mul_i32 %[t2], %[cc], 229376\n\t"
      "s_add_u32 m0, %[ld0], 8192\n\t"
      "s_add_u32 %[t1], %[t2], 0\n\t"
      "buffer_load_dwordx4 v4, %[rb], %[t1] offen lds\n\t"
      "s_add_u32 m0, %[ld0], 9216\n\t"
      "s_add_u32 %[t1], %[t2], 28672\n\t"
      "buffer_load_dwordx4 v4, %[rb], %[t1] offen lds\n\t"
      "s_add_u32 %[cc], %[rot], 2\n\t"
      "s_and_b32 %[cc], %[cc], 7\n\t"
      "s_mul_i32 %[t2], %[cc], 229376\n\t"
      "s_add_u32 m0, %[ld0], 16384\n\t"
      "s_add_u32 %[t1], %[t2], 0\n\t"
      "buffer_load_dwordx4 v4, %[rb], %[t1] offen lds\n\t"
      "s_add_u32 m0, %[ld0], 17408\n\t"
      "s_add_u32 %[t1], %[t2], 28672\n\t"
      "buffer_load_dwordx4 v4, %[rb], %[t1] offen lds\n\t"
      "s_add_u32 %[cc], %[rot], 3\n\t"
      "s_and_b32 %[cc], %[cc], 7\n\t"
      "s_mul_i32 %[t2], %[cc], 229376\n\t"
      "s_add_u32 m0, %[ld0], 24576\n\t"
      "s_add_u32 %[t1], %[t2], 0\n\t"
      "buffer_load_dwordx4 v4, %[rb], %[t1] offen lds\n\t"
      "s_add_u32 m0, %[ld0], 25600\n\t"
      "s_add_u32 %[t1], %[t2], 28672\n\t"
      "buffer_load_dwordx4 v4, %[rb], %[t1] offen lds\n\t"
      "s_waitcnt vmcnt(16)\n\t"
      "s_barrier\n\t"
      "s_add_u32 %[cc], %[rot], 4\n\t"
      "s_and_b32 %[cc], %[cc], 7\n\t"
      "s_mul_i32 %[t2], %[cc], 229376\n\t"
      "s_add_u32 m0, %[ld0], 32768\n\t"
      "s_add_u32 %[t1], %[t2], 0\n\t"
      "buffer_load_dwordx4 v4, %[rb], %[t1] offen lds\n\t"
      "s_add_u32 m0, %[ld0], 33792\n\t"
      "s_add_u32 %[t1], %[t2], 28672\n\t"
      "buffer_load_dwordx4 v4, %[rb], %[t1] offen lds\n\t"
      "s_add_u32 %[cc], %[rot], 5\n\t"
      "s_and_b32 %[cc], %[cc], 7\n\t"
      "s_mul_i32 %[t2], %[cc], 229376\n\t"
      "s_add_u32 m0, %[ld0], 40960\n\t"
      "s_add_u32 %[t1], %[t2], 0\n\t"
      "buffer_load_dwordx4 v4, %[rb], %[t1] offen lds\n\t"
      "s_add_u32 m0, %[ld0], 41984\n\t"
      "s_add_u32 %[t1], %[t2], 28672\n\t"
      "buffer_load_dwordx4 v4, %[rb], %[t1] offen lds\n\t"
      "s_add_u32 %[cc], %[rot], 6\n\t"
      "s_and_b32 %[cc], %[cc], 7\n\t"
      "s_mul_i32 %[t2], %[cc], 229376\n\t"
      "s_add_u32 m0, %[ld0], 49152\n\t"
      "s_add_u32 %[t1], %[t2], 0\n\t"
      "buffer_load_dwordx4 v4, %[rb], %[t1] offen lds\n\t"
      "s_add_u32 m0, %[ld0], 50176\n\t"
      "s_add_u32 %[t1], %[t2], 28672\n\t"
      "buffer_load_dwordx4 v4, %[rb], %[t1] offen lds\n\t"
      "s_add_u32 %[cc], %[rot], 7\n\t"
      "s_and_b32 %[cc], %[cc], 7\n\t"
      "s_mul_i32 %[t2], %[cc], 229376\n\t"
      "s_add_u32 m0, %[ld0], 57344\n\t"
      "s_add_u32 %[t1], %[t2], 0\n\t"
      "buffer_load_dwordx4 v4, %[rb], %[t1] offen lds\n\t"
      "s_add_u32 m0, %[ld0], 58368\n\t"
      "s_add_u32 %[t1], %[t2], 28672\n\t"
      "buffer_load_dwordx4 v4, %[rb], %[t1] offen lds\n\t"
      "s_waitcnt vmcnt(16)\n\t"
      "s_barrier\n\t"
      "s_waitcnt vmcnt(8)\n\t"
      "s_barrier\n\t"
      "buffer_load_dword %[mw], %[pa], %[rs], 0 offen sc1\n\t"
      "s_waitcnt vmcnt(1)\n\t"
      "s_barrier\n\t"
      "s_lshr_b32 %[t1], %[ld0], 1\n\t"
      "s_add_u32 %[cc], %[rot], 0\n\t"
      "s_and_b32 %[cc], %[cc], 7\n\t"
      "s_add_u32 m0, %[t1], 65536\n\t"
      "s_mul_i32 %[t2], %[cc], 229376\n\t"
      "buffer_load_dwordx4 v5, %[rb], %[t2] offen lds\n\t"
      "s_add_u32 %[cc], %[rot], 1\n\t"
      "s_and_b32 %[cc], %[cc], 7\n\t"
      "s_add_u32 m0, %[t1], 69632\n\t"
      "s_mul_i32 %[t2], %[cc], 229376\n\t"
      "buffer_load_dwordx4 v5, %[rb], %[t2] offen lds\n\t"
      "s_add_u32 %[cc], %[rot], 2\n\t"
      "s_and_b32 %[cc], %[cc], 7\n\t"
      "s_add_u32 m0, %[t1], 73728\n\t"
      "s_mul_i32 %[t2], %[cc], 229376\n\t"
      "buffer_load_dwordx4 v5, %[rb], %[t2] offen lds\n\t"
      "s_add_u32 %[cc], %[rot], 3\n\t"
      "s_and_b32 %[cc], %[cc], 7\n\t"
      "s_add_u32 m0, %[t1], 77824\n\t"
      "s_mul_i32 %[t2], %[cc], 229376\n\t"
      "buffer_load_dwordx4 v5, %[rb], %[t2] offen lds\n\t"
      "s_add_u32 %[cc], %[rot], 4\n\t"
      "s_and_b32 %[cc], %[cc], 7\n\t"
      "s_add_u32 m0, %[t1], 81920\n\t"
      "s_mul_i32 %[t2], %[cc], 229376\n\t"
      "buffer_load_dwordx4 v5, %[rb], %[t2] offen lds\n\t"
      "s_add_u32 %[cc], %[rot], 5\n\t"
      "s_and_b32 %[cc], %[cc], 7\n\t"
      "s_add_u32 m0, %[t1], 86016\n\t"
      "s_mul_i32 %[t2], %[cc], 229376\n\t"
      "buffer_load_dwordx4 v5, %[rb], %[t2] offen lds\n\t"
      "s_add_u32 %[cc], %[rot], 6\n\t"
      "s_and_b32 %[cc], %[cc], 7\n\t"
      "s_add_u32 m0, %[t1], 90112\n\t"
      "s_mul_i32 %[t2], %[cc], 229376\n\t"
      "buffer_load_dwordx4 v5, %[rb], %[t2] offen lds\n\t"
      "s_add_u32 %[cc], %[rot], 7\n\t"
      "s_and_b32 %[cc], %[cc], 7\n\t"
      "s_add_u32 m0, %[t1], 94208\n\t"
      "s_mul_i32 %[t2], %[cc], 229376\n\t"
      "buffer_load_dwordx4 v5, %[rb], %[t2] offen lds\n\t"
      "s_waitcnt vmcnt(8)\n\t"
          L2OPS);
    tok = *(const LAS unsigned*)(lds0 + L_TOK);
    msg_v = ((mw >> 8) & 63) | (tok << 8) | 0x10000u;
    if (odd) {
      const int row = r2 * 128 + cg * 16 + kq * 4 + (lane >> 4);
      float z[4] = {0.f, 0.f, 0.f, 0.f};
#pragma unroll
      for (int c = 0; c < 4; c++) {
        const u32x4 xv = *(const GAS u32x4*)(x + (size_t)row * 512 + c * 128 + (lane & 15) * 8);
        const u32x4 gv = *(const GAS u32x4*)(gw + c * 128 + (lane & 15) * 8);
#pragma unroll
        for (int j = 0; j < 4; j++) {
          const unsigned xa = xv[j], ga = gv[j];
          z[j] = __builtin_amdgcn_fdot2_f32_bf16(__builtin_bit_cast(bf16x2, xa), __builtin_bit_cast(bf16x2, ga), z[j], false);
        }
      }
      float t = (z[0] + z[1]) + (z[2] + z[3]);
      t += __uint_as_float(__builtin_amdgcn_mov_dpp(__float_as_uint(t), 0x128, 0xF, 0xF, true));
      t += __uint_as_float(__builtin_amdgcn_mov_dpp(__float_as_uint(t), 0x124, 0xF, 0xF, true));
      t += __uint_as_float(__builtin_amdgcn_mov_dpp(__float_as_uint(t), 0x122, 0xF, 0xF, true));
      t += __uint_as_float(__builtin_amdgcn_mov_dpp(__float_as_uint(t), 0x121, 0xF, 0xF, true));
      if ((lane & 15) == 0) WST1(__float_as_uint(__builtin_amdgcn_rcpf(1.f + __builtin_amdgcn_exp2f(-LOG2E * t))), WS_G + row * 4);
    }
    BARRIER();
    WST1(msg_v, ma);
    if (!odd) asm volatile("s_waitcnt vmcnt(1)\n\ts_barrier" ::: "memory");
    if (!odd) {
      da[0] = WS_OF + ((4 * r2 + kq) * 32 + 4 * cg) * 1024 + l16;
      da[1] = da[0] + 2048;
    }
#pragma unroll
    for (int r = 0; r < 16; r++) dd[0][r] = __uint_as_float(~0u);
    auto first = [&]() {
    asm volatile(
      "s_add_u32 m0, %[l0], 0\n\t"
      "s_add_u32 %[t1], %[kb], 0\n\t"
      "buffer_load_dwordx4 %[l16], %[rs], %[t1] offen sc1 lds\n\t"
      "s_add_u32 m0, %[l0], 151552\n\t"
      "s_add_u32 %[t1], %[qb], 0\n\t"
      "buffer_load_dwordx4 %[l16], %[rs], %[t1] offen sc1 lds\n\t"
      "s_add_u32 m0, %[l0], 8192\n\t"
      "s_add_u32 %[t1], %[kb], 2097152\n\t"
      "buffer_load_dwordx4 %[l16], %[rs], %[t1] offen sc1 lds\n\t"
      "s_add_u32 m0, %[l0], 1024\n\t"
      "s_add_u32 %[t1], %[kb], 1024\n\t"
      "buffer_load_dwordx4 %[l16], %[rs], %[t1] offen sc1 lds\n\t"
      "s_add_u32 m0, %[l0], 152576\n\t"
      "s_add_u32 %[t1], %[qb], 1024\n\t"
      "buffer_load_dwordx4 %[l16], %[rs], %[t1] offen sc1 lds\n\t"
      "s_add_u32 m0, %[l0], 9216\n\t"
      "s_add_u32 %[t1], %[kb], 2098176\n\t"
      "buffer_load_dwordx4 %[l16], %[rs], %[t1] offen sc1 lds\n\t"
        : [t1] "=&s"(t1)
        : [l16] "v"(l16), [rs] "s"(rs), [l0] "s"(RFL(lds0 + kq * 2048)), [kb] "s"(RFL(WS_KF + head * 65536 + kq * 2048)),
          [qb] "s"(RFL(WS_QF + (head * 64 + qt0) * 1024))
        : "memory", "scc");
    };
    __builtin_amdgcn_s_sleep(2);
    const unsigned peek = __builtin_amdgcn_raw_buffer_load_b32(
        rs, (lane < 32 ? WS_KF + (lane >> 4) * 0x200000 + head * 65536 + kq * 2048 : WS_QF + (head * 64 + qt0) * 1024) + (lane & 15) * 128, 0, 16);
    const unsigned fm = lane < 32 && (wid == 7 || !(lane & 1) || (lane >> 2) == (rb >> 2)) ? 0x1003Fu : 0u, fe = (tok | 0x10000u) & fm;
    const unsigned fa = fm ? inbox : OOR;
#pragma unroll 1
    for (int spin = 0; spin < (1 << 16); spin++) {
      mw = __builtin_amdgcn_raw_buffer_load_b32(rs, fa, 0, 16);
      const unsigned nv = fm ? ((mw >> 8) & 63) | (msg_v & ~63u) : msg_v;
      if (__any(nv != msg_v)) { msg_v = nv; WST1(msg_v, ma); }
      if (__all((mw & fm) == fe)) break;
      __builtin_amdgcn_s_sleep(4);
      asm volatile("" ::: "memory");
    }
    asm volatile("" ::"v"(peek));
    first();
  } else {
    {
      const int cs = (3 - (int)((unsigned long long)wqkv >> 7)) & 7, ln = lane_now();
      if (wid == 1 && (cs >> 2) == (cg >> 2))
        asm volatile("s_mov_b32 m0, %2\n\ts_nop 0\n\tglobal_load_lds_dword %0, %1" ::"v"((16 * (b >> 3) + (ln & 15)) * 3072 + min(ln >> 4, 2) * 1024 + cs * 128),
                     "s"(wqkv), "s"(lds0 + L_TOK + 256) : "memory");
    }
    u32x4 xa, wa;
    auto addr = [&]() {
      LANE();
#pragma unroll
      for (int j = 0; j < 4; j++) xa[j] = lds0 + (32 * kq + l31) * 128 + (((2 * j + hi) ^ ((l31 >> 1) & 7)) << 4);
      const int q4 = (lane & 15) >> 2;
      const unsigned w0 = lds0 + (8 * hi + q4) * 128 + ((q4 >> 1) << 6) + ((lane >> 4) & 1) * 32 + (lane & 3) * 8;
      wa = (u32x4){w0, w0 ^ 64, w0, 0};
    };
    RS();
    f32x16 xr[8], acc[4];
#define XOUT "=&{v[0:15]}"(xr[0]), "=&{v[16:31]}"(xr[1]), "=&{v[32:47]}"(xr[2]), "=&{v[48:63]}"(xr[3]), "=&{v[64:79]}"(xr[4]), \
             "=&{v[80:95]}"(xr[5]), "=&{v[96:111]}"(xr[6]), "=&{v[112:127]}"(xr[7])
#define XIN "{v[0:15]}"(xr[0]), "{v[16:31]}"(xr[1]), "{v[32:47]}"(xr[2]), "{v[48:63]}"(xr[3]), "{v[64:79]}"(xr[4]), \
            "{v[80:95]}"(xr[5]), "{v[96:111]}"(xr[6]), "{v[112:127]}"(xr[7])
#define P2CLOB "v224", "v225", "v226", "v227", "v232", "v233", "v234", "v235", "v236", "v237", "v238", "v239", "v240", "v241", "v242", "v243", "v244", "v245", "v246", "v247",
#define WCLOB "memory", \
    "v192", "v193", "v194", "v195", "v196", "v197", "v198", "v199", "v200", "v201", "v202", "v203", "v204", "v205", "v206", "v207", "v208", "v209", "v210", "v211", "v212", "v213", "v214", "v215", "v216", "v217", "v218", "v219", "v220", "v221", "v222", "v223"
#define P1OPS                                                                                                          \
  : XOUT, "=&{v[128:143]}"(acc[0]), "=&{v[144:159]}"(acc[1]), "=&{v[160:175]}"(acc[2]), "=&{v[176:191]}"(acc[3]),      \
    "+{v[224:227]}"(xa), "+{v[228:231]}"(wa) :: WCLOB
    const int tt = 4 * r2 + kq;
    const unsigned hb = (4 * cg * 64 + tt) * 1024, dh0 = WS_HF + (tt * 112 + 14 * cg - 2 * par) * 1024;
    {
      LANE();
      const int q = (lane & 15) >> 2, p = lane & 3;
      const unsigned o2 = (lane & 11) == 0 ? 0x3F803F80u : 0u;
      *(LAS u32x4*)(lds0 + L_ONES + kq * 1024 + l16) = (u32x4){o2, o2, o2, o2};
      const bool nd = (lane >> 4) == 2 || (lane & 61) == (rb & 28);
      const unsigned v0 = (lane >> 3) * 1024 + (((lane & 7) ^ ((lane >> 2) & 4)) << 4);
      const unsigned va = lds0 + (l31 < 16 ? 8192 : L_ONES) + hi * 256 + (l31 & 15) * 16;
      const unsigned wa0 = (lds0 + 16384 + kq * 8192 + (4 * hi + q) * 128 + ((q >> 1) << 6) + ((lane >> 4) & 1) * 32 + (((p & 1) << 1) | (p >> 1)) * 8) ^ (kq * 32);
      const unsigned pk = lds0 + L_PARK + kq * 4096 + l16;
      *(LAS u32x4*)(pk) = (u32x4){lds0 + l16, va, wa0, wa0 ^ 64};
      *(LAS u32x4*)(pk + 1024) = (u32x4){lds0 + l16 + 4096, l31 < 16 ? va + 4096 : va, wa0, wa0 ^ 64};
      *(LAS u32x4*)(pk + 2048) = (u32x4){v0, v0 + 8192, v0 + 16384, v0 + 24576};
      *(LAS u32x4*)(pk + 3072) = (u32x4){l31 < 16 ? 1u : 0u, nd ? 0x2003Fu : 0u, 0u, INBOX(lane)};
      asm volatile("s_waitcnt lgkmcnt(0)" ::: "memory");
    }
    if (odd) {
      addr();
      asm volatile(
      "s_barrier\n\t"
      "ds_read_b128 v[0:3], v224\n\t"
      "ds_read_b64_tr_b16 v[192:193], v228 offset:16384\n\t"
      "ds_read_b64_tr_b16 v[194:195], v228 offset:16896\n\t"
      "ds_read_b64_tr_b16 v[196:197], v229 offset:16384\n\t"
      "ds_read_b64_tr_b16 v[198:199], v229 offset:16896\n\t"
      "ds_read_b64_tr_b16 v[200:201], v228 offset:24576\n\t"
      "ds_read_b64_tr_b16 v[202:203], v228 offset:25088\n\t"
      "ds_read_b64_tr_b16 v[204:205], v229 offset:24576\n\t"
      "ds_read_b64_tr_b16 v[206:207], v229 offset:25088\n\t"
      "s_waitcnt lgkmcnt(4)\n\t"
      "v_mfma_f32_32x32x16_bf16 v[128:143], v[192:195], v[0:3], 0\n\t"
      "ds_read_b128 v[4:7], v225\n\t"
      "ds_read_b64_tr_b16 v[208:209], v228 offset:18432\n\t"
      "ds_read_b64_tr_b16 v[210:211], v228 offset:18944\n\t"
      "v_mfma_f32_32x32x16_bf16 v[144:159], v[196:199], v[0:3], 0\n\t"
      "ds_read_b64_tr_b16 v[212:213], v229 offset:18432\n\t"
      "ds_read_b64_tr_b16 v[214:215], v229 offset:18944\n\t"
      "ds_read_b64_tr_b16 v[216:217], v228 offset:26624\n\t"
      "s_waitcnt lgkmcnt(6)\n\t"
      "v_mfma_f32_32x32x16_bf16 v[160:175], v[200:203], v[0:3], 0\n\t"
      "ds_read_b64_tr_b16 v[218:219], v228 offset:27136\n\t"
      "ds_read_b64_tr_b16 v[220:221], v229 offset:26624\n\t"
      "ds_read_b64_tr_b16 v[222:223], v229 offset:27136\n\t"
      "v_mfma_f32_32x32x16_bf16 v[176:191], v[204:207], v[0:3], 0\n\t"
      "s_waitcnt lgkmcnt(4)\n\t"
      "v_mfma_f32_32x32x16_bf16 v[128:143], v[208:211], v[4:7], v[128:143]\n\t"
      "ds_read_b128 v[8:11], v226\n\t"
      "ds_read_b64_tr_b16 v[192:193], v228 offset:20480\n\t"
      "ds_read_b64_tr_b16 v[194:195], v228 offset:20992\n\t"
      "v_mfma_f32_32x32x16_bf16 v[144:159], v[212:215], v[4:7], v[144:159]\n\t"
      "ds_read_b64_tr_b16 v[196:197], v229 offset:20480\n\t"
      "ds_read_b64_tr_b16 v[198:199], v229 offset:20992\n\t"
      "ds_read_b64_tr_b16 v[200:201], v228 offset:28672\n\t"
      "s_waitcnt lgkmcnt(6)\n\t"
      "v_mfma_f32_32x32x16_bf16 v[160:175], v[216:219], v[4:7], v[160:175]\n\t"
      "ds_read_b64_tr_b16 v[202:203], v228 offset:29184\n\t"
      "ds_read_b64_tr_b16 v[204:205], v229 offset:28672\n\t"
      "ds_read_b64_tr_b16 v[206:207], v229 offset:29184\n\t"
      "v_mfma_f32_32x32x16_bf16 v[176:191], v[220:223], v[4:7], v[176:191]\n\t"
      "s_waitcnt lgkmcnt(4)\n\t"
      "v_mfma_f32_32x32x16_bf16 v[128:143], v[192:195], v[8:11], v[128:143]\n\t"
      "ds_read_b128 v[12:15], v227\n\t"
      "ds_read_b64_tr_b16 v[208:209], v228 offset:22528\n\t"
      "ds_read_b64_tr_b16 v[210:211], v228 offset:23040\n\t"
      "v_mfma_f32_32x32x16_bf16 v[144:159], v[196:199], v[8:11], v[144:159]\n\t"
      "ds_read_b64_tr_b16 v[212:213], v229 offset:22528\n\t"
      "ds_read_b64_tr_b16 v[214:215], v229 offset:23040\n\t"
      "ds_read_b64_tr_b16 v[216:217], v228 offset:30720\n\t"
      "s_waitcnt lgkmcnt(6)\n\t"
      "v_mfma_f32_32x32x16_bf16 v[160:175], v[200:203], v[8:11], v[160:175]\n\t"
      "ds_read_b64_tr_b16 v[218:219], v228 offset:31232\n\t"
      "ds_read_b64_tr_b16 v[220:221], v229 offset:30720\n\t"
      "ds_read_b64_tr_b16 v[222:223], v229 offset:31232\n\t"
      "v_mfma_f32_32x32x16_bf16 v[176:191], v[204:207], v[8:11], v[176:191]\n\t"
      "v_add_u32 v224, 32768, v224\n\t"
      "v_add_u32 v225, 32768, v225\n\t"
      "v_add_u32 v226, 32768, v226\n\t"
      "v_add_u32 v227, 32768, v227\n\t"
      "s_waitcnt lgkmcnt(4)\n\t"
      "v_mfma_f32_32x32x16_bf16 v[128:143], v[208:211], v[12:15], v[128:143]\n\t"
      "v_add_u32 v228, 32768, v228\n\t"
      "v_add_u32 v229, 32768, v229\n\t"
      "s_waitcnt lgkmcnt(0)\n\t"
      "s_barrier\n\t"
      "ds_read_b128 v[16:19], v224\n\t"
      "ds_read_b64_tr_b16 v[192:193], v228 offset:16384\n\t"
      "ds_read_b64_tr_b16 v[194:195], v228 offset:16896\n\t"
      "v_mfma_f32_32x32x16_bf16 v[144:159], v[212:215], v[12:15], v[144:159]\n\t"
      "ds_read_b64_tr_b16 v[196:197], v229 offset:16384\n\t"
      "ds_read_b64_tr_b16 v[198:199], v229 offset:16896\n\t"
      "ds_read_b64_tr_b16 v[200:201], v228 offset:24576\n\t"
      "v_mfma_f32_32x32x16_bf16 v[160:175], v[216:219], v[12:15], v[160:175]\n\t"
      "ds_read_b64_tr_b16 v[202:203], v228 offset:25088\n\t"
      "ds_read_b64_tr_b16 v[204:205], v229 offset:24576\n\t"
      "ds_read_b64_tr_b16 v[206:207], v229 offset:25088\n\t"
      "v_mfma_f32_32x32x16_bf16 v[176:191], v[220:223], v[12:15], v[176:191]\n\t"
      "s_waitcnt lgkmcnt(4)\n\t"
      "v_mfma_f32_32x32x16_bf16 v[128:143], v[192:195], v[16:19], v[128:143]\n\t"
      "ds_read_b128 v[20:23], v225\n\t"
      "ds_read_b64_tr_b16 v[208:209], v228 offset:18432\n\t"
      "ds_read_b64_tr_b16 v[210:211], v228 offset:18944\n\t"
      "v_mfma_f32_32x32x16_bf16 v[144:159], v[196:199], v[16:19], v[144:159]\n\t"
      "ds_read_b64_tr_b16 v[212:213], v229 offset:18432\n\t"
      "ds_read_b64_tr_b16 v[214:215], v229 offset:18944\n\t"
      "ds_read_b64_tr_b16 v[216:217], v228 offset:26624\n\t"
      "s_waitcnt lgkmcnt(6)\n\t"
      "v_mfma_f32_32x32x16_bf16 v[160:175], v[200:203], v[16:19], v[160:175]\n\t"
      "ds_read_b64_tr_b16 v[218:219], v228 offset:27136\n\t"
      "ds_read_b64_tr_b16 v[220:221], v229 offset:26624\n\t"
      "ds_read_b64_tr_b16 v[222:223], v229 offset:27136\n\t"
      "v_mfma_f32_32x32x16_bf16 v[176:191], v[204:207], v[16:19], v[176:191]\n\t"
      "s_waitcnt lgkmcnt(4)\n\t"
      "v_mfma_f32_32x32x16_bf16 v[128:143], v[208:211], v[20:23], v[128:143]\n\t"
      "ds_read_b128 v[24:27], v226\n\t"
      "ds_read_b64_tr_b16 v[192:193], v228 offset:20480\n\t"
      "ds_read_b64_tr_b16 v[194:195], v228 offset:20992\n\t"
      "v_mfma_f32_32x32x16_bf16 v[144:159], v[212:215], v[20:23], v[144:159]\n\t"
      "ds_read_b64_tr_b16 v[196:197], v229 offset:20480\n\t"
      "ds_read_b64_tr_b16 v[198:199], v229 offset:20992\n\t"
      "ds_read_b64_tr_b16 v[200:201], v228 offset:28672\n\t"
      "s_waitcnt lgkmcnt(6)\n\t"
      "v_mfma_f32_32x32x16_bf16 v[160:175], v[216:219], v[20:23], v[160:175]\n\t"
      "ds_read_b64_tr_b16 v[202:203], v228 offset:29184\n\t"
      "ds_read_b64_tr_b16 v[204:205], v229 offset:28672\n\t"
      "ds_read_b64_tr_b16 v[206:207], v229 offset:29184\n\t"
      "v_mfma_f32_32x32x16_bf16 v[176:191], v[220:223], v[20:23], v[176:191]\n\t"
      "s_waitcnt lgkmcnt(4)\n\t"
      "v_mfma_f32_32x32x16_bf16 v[128:143], v[192:195], v[24:27], v[128:143]\n\t"
      "ds_read_b128 v[28:31], v227\n\t"
      "ds_read_b64_tr_b16 v[208:209], v228 offset:22528\n\t"
      "ds_read_b64_tr_b16 v[210:211], v228 offset:23040\n\t"
      "v_mfma_f32_32x32x16_bf16 v[144:159], v[196:199], v[24:27], v[144:159]\n\t"
      "ds_read_b64_tr_b16 v[212:213], v229 offset:22528\n\t"
      "ds_read_b64_tr_b16 v[214:215], v229 offset:23040\n\t"
      "ds_read_b64_tr_b16 v[216:217], v228 offset:30720\n\t"
      "s_waitcnt lgkmcnt(6)\n\t"
      "v_mfma_f32_32x32x16_bf16 v[160:175], v[200:203], v[24:27], v[160:175]\n\t"
      "ds_read_b64_tr_b16 v[218:219], v228 offset:31232\n\t"
      "ds_read_b64_tr_b16 v[220:221], v229 offset:30720\n\t"
      "ds_read_b64_tr_b16 v[222:223], v229 offset:31232\n\t"
      "v_mfma_f32_32x32x16_bf16 v[176:191], v[204:207], v[24:27], v[176:191]\n\t"
      "v_add_u32 v224, 32768, v224\n\t"
      "v_add_u32 v225, 32768, v225\n\t"
      "v_add_u32 v226, 32768, v226\n\t"
      "v_add_u32 v227, 32768, v227\n\t"
      "s_waitcnt lgkmcnt(4)\n\t"
      "v_mfma_f32_32x32x16_bf16 v[128:143], v[208:211], v[28:31], v[128:143]\n\t"
      "v_add_u32 v228, 32768, v228\n\t"
      "v_add_u32 v229, 32768, v229\n\t"
      "s_waitcnt lgkmcnt(0)\n\t"
      "s_barrier\n\t"
      "ds_read_b128 v[32:35], v224\n\t"
      "ds_read_b64_tr_b16 v[192:193], v228 offset:16384\n\t"
      "ds_read_b64_tr_b16 v[194:195], v228 offset:16896\n\t"
      "v_mfma_f32_32x32x16_bf16 v[144:159], v[212:215], v[28:31], v[144:159]\n\t"
      "ds_read_b64_tr_b16 v[196:197], v229 offset:16384\n\t"
      "ds_read_b64_tr_b16 v[198:199], v229 offset:16896\n\t"
      "ds_read_b64_tr_b16 v[200:201], v228 offset:24576\n\t"
      "v_mfma_f32_32x32x16_bf16 v[160:175], v[216:219], v[28:31], v[160:175]\n\t"
      "ds_read_b64_tr_b16 v[202:203], v228 offset:25088\n\t"
      "ds_read_b64_tr_b16 v[204:205], v229 offset:24576\n\t"
      "ds_read_b64_tr_b16 v[206:207], v229 offset:25088\n\t"
      "v_mfma_f32_32x32x16_bf16 v[176:191], v[220:223], v[28:31], v[176:191]\n\t"
      "s_waitcnt lgkmcnt(4)\n\t"
      "v_mfma_f32_32x32x16_bf16 v[128:143], v[192:195], v[32:35], v[128:143]\n\t"
      "ds_read_b128 v[36:39], v225\n\t"
      "ds_read_b64_tr_b16 v[208:209], v228 offset:18432\n\t"
      "ds_read_b64_tr_b16 v[210:211], v228 offset:18944\n\t"
      "v_mfma_f32_32x32x16_bf16 v[144:159], v[196:199], v[32:35], v[144:159]\n\t"
      "ds_read_b64_tr_b16 v[212:213], v229 offset:18432\n\t"
      "ds_read_b64_tr_b16 v[214:215], v229 offset:18944\n\t"
      "ds_read_b64_tr_b16 v[216:217], v228 offset:26624\n\t"
      "s_waitcnt lgkmcnt(6)\n\t"
      "v_mfma_f32_32x32x16_bf16 v[160:175], v[200:203], v[32:35], v[160:175]\n\t"
      "ds_read_b64_tr_b16 v[218:219], v228 offset:27136\n\t"
      "ds_read_b64_tr_b16 v[220:221], v229 offset:26624\n\t"
      "ds_read_b64_tr_b16 v[222:223], v229 offset:27136\n\t"
      "v_mfma_f32_32x32x16_bf16 v[176:191], v[204:207], v[32:35], v[176:191]\n\t"
      "s_waitcnt lgkmcnt(4)\n\t"
      "v_mfma_f32_32x32x16_bf16 v[128:143], v[208:211], v[36:39], v[128:143]\n\t"
      "ds_read_b128 v[40:43], v226\n\t"
      "ds_read_b64_tr_b16 v[192:193], v228 offset:20480\n\t"
      "ds_read_b64_tr_b16 v[194:195], v228 offset:20992\n\t"
      "v_mfma_f32_32x32x16_bf16 v[144:159], v[212:215], v[36:39], v[144:159]\n\t"
      "ds_read_b64_tr_b16 v[196:197], v229 offset:20480\n\t"
      "ds_read_b64_tr_b16 v[198:199], v229 offset:20992\n\t"
      "ds_read_b64_tr_b16 v[200:201], v228 offset:28672\n\t"
      "s_waitcnt lgkmcnt(6)\n\t"
      "v_mfma_f32_32x32x16_bf16 v[160:175], v[216:219], v[36:39], v[160:175]\n\t"
      "ds_read_b64_tr_b16 v[202:203], v228 offset:29184\n\t"
      "ds_read_b64_tr_b16 v[204:205], v229 offset:28672\n\t"
      "ds_read_b64_tr_b16 v[206:207], v229 offset:29184\n\t"
      "v_mfma_f32_32x32x16_bf16 v[176:191], v[220:223], v[36:39], v[176:191]\n\t"
      "s_waitcnt lgkmcnt(4)\n\t"
      "v_mfma_f32_32x32x16_bf16 v[128:143], v[192:195], v[40:43], v[128:143]\n\t"
      "ds_read_b128 v[44:47], v227\n\t"
      "ds_read_b64_tr_b16 v[208:209], v228 offset:22528\n\t"
      "ds_read_b64_tr_b16 v[210:211], v228 offset:23040\n\t"
      "v_mfma_f32_32x32x16_bf16 v[144:159], v[196:199], v[40:43], v[144:159]\n\t"
      "ds_read_b64_tr_b16 v[212:213], v229 offset:22528\n\t"
      "ds_read_b64_tr_b16 v[214:215], v229 offset:23040\n\t"
      "ds_read_b64_tr_b16 v[216:217], v228 offset:30720\n\t"
      "s_waitcnt lgkmcnt(6)\n\t"
      "v_mfma_f32_32x32x16_bf16 v[160:175], v[200:203], v[40:43], v[160:175]\n\t"
      "ds_read_b64_tr_b16 v[218:219], v228 offset:31232\n\t"
      "ds_read_b64_tr_b16 v[220:221], v229 offset:30720\n\t"
      "ds_read_b64_tr_b16 v[222:223], v229 offset:31232\n\t"
      "v_mfma_f32_32x32x16_bf16 v[176:191], v[204:207], v[40:43], v[176:191]\n\t"
      "v_add_u32 v224, 32768, v224\n\t"
      "v_add_u32 v225, 32768, v225\n\t"
      "v_add_u32 v226, 32768, v226\n\t"
      "v_add_u32 v227, 32768, v227\n\t"
      "s_waitcnt lgkmcnt(4)\n\t"
      "v_mfma_f32_32x32x16_bf16 v[128:143], v[208:211], v[44:47], v[128:143]\n\t"
      "v_add_u32 v228, 32768, v228\n\t"
      "v_add_u32 v229, 32768, v229\n\t"
      "s_waitcnt lgkmcnt(0)\n\t"
      "s_barrier\n\t"
      "ds_read_b128 v[48:51], v224\n\t"
      "ds_read_b64_tr_b16 v[192:193], v228 offset:16384\n\t"
      "ds_read_b64_tr_b16 v[194:195], v228 offset:16896\n\t"
      "v_mfma_f32_32x32x16_bf16 v[144:159], v[212:215], v[44:47], v[144:159]\n\t"
      "ds_read_b64_tr_b16 v[196:197], v229 offset:16384\n\t"
      "ds_read_b64_tr_b16 v[198:199], v229 offset:16896\n\t"
      "ds_read_b64_tr_b16 v[200:201], v228 offset:24576\n\t"
      "v_mfma_f32_32x32x16_bf16 v[160:175], v[216:219], v[44:47], v[160:175]\n\t"
      "ds_read_b64_tr_b16 v[202:203], v228 offset:25088\n\t"
      "ds_read_b64_tr_b16 v[204:205], v229 offset:24576\n\t"
      "ds_read_b64_tr_b16 v[206:207], v229 offset:25088\n\t"
      "v_mfma_f32_32x32x16_bf16 v[176:191], v[220:223], v[44:47], v[176:191]\n\t"
      "s_waitcnt lgkmcnt(4)\n\t"
      "v_mfma_f32_32x32x16_bf16 v[128:143], v[192:195], v[48:51], v[128:143]\n\t"
      "ds_read_b128 v[52:55], v225\n\t"
      "ds_read_b64_tr_b16 v[208:209], v228 offset:18432\n\t"
      "ds_read_b64_tr_b16 v[210:211], v228 offset:18944\n\t"
      "v_mfma_f32_32x32x16_bf16 v[144:159], v[196:199], v[48:51], v[144:159]\n\t"
      "ds_read_b64_tr_b16 v[212:213], v229 offset:18432\n\t"
      "ds_read_b64_tr_b16 v[214:215], v229 offset:18944\n\t"
      "ds_read_b64_tr_b16 v[216:217], v228 offset:26624\n\t"
      "s_waitcnt lgkmcnt(6)\n\t"
      "v_mfma_f32_32x32x16_bf16 v[160:175], v[200:203], v[48:51], v[160:175]\n\t"
      "ds_read_b64_tr_b16 v[218:219], v228 offset:27136\n\t"
      "ds_read_b64_tr_b16 v[220:221], v229 offset:26624\n\t"
      "ds_read_b64_tr_b16 v[222:223], v229 offset:27136\n\t"
      "v_mfma_f32_32x32x16_bf16 v[176:191], v[204:207], v[48:51], v[176:191]\n\t"
      "s_waitcnt lgkmcnt(4)\n\t"
      "v_mfma_f32_32x32x16_bf16 v[128:143], v[208:211], v[52:55], v[128:143]\n\t"
      "ds_read_b128 v[56:59], v226\n\t"
      "ds_read_b64_tr_b16 v[192:193], v228 offset:20480\n\t"
      "ds_read_b64_tr_b16 v[194:195], v228 offset:20992\n\t"
      "v_mfma_f32_32x32x16_bf16 v[144:159], v[212:215], v[52:55], v[144:159]\n\t"
      "ds_read_b64_tr_b16 v[196:197], v229 offset:20480\n\t"
      "ds_read_b64_tr_b16 v[198:199], v229 offset:20992\n\t"
      "ds_read_b64_tr_b16 v[200:201], v228 offset:28672\n\t"
      "s_waitcnt lgkmcnt(6)\n\t"
      "v_mfma_f32_32x32x16_bf16 v[160:175], v[216:219], v[52:55], v[160:175]\n\t"
      "ds_read_b64_tr_b16 v[202:203], v228 offset:29184\n\t"
      "ds_read_b64_tr_b16 v[204:205], v229 offset:28672\n\t"
      "ds_read_b64_tr_b16 v[206:207], v229 offset:29184\n\t"
      "v_mfma_f32_32x32x16_bf16 v[176:191], v[220:223], v[52:55], v[176:191]\n\t"
      "s_waitcnt lgkmcnt(4)\n\t"
      "v_mfma_f32_32x32x16_bf16 v[128:143], v[192:195], v[56:59], v[128:143]\n\t"
      "ds_read_b128 v[60:63], v227\n\t"
      "ds_read_b64_tr_b16 v[208:209], v228 offset:22528\n\t"
      "ds_read_b64_tr_b16 v[210:211], v228 offset:23040\n\t"
      "v_mfma_f32_32x32x16_bf16 v[144:159], v[196:199], v[56:59], v[144:159]\n\t"
      "ds_read_b64_tr_b16 v[212:213], v229 offset:22528\n\t"
      "ds_read_b64_tr_b16 v[214:215], v229 offset:23040\n\t"
      "ds_read_b64_tr_b16 v[216:217], v228 offset:30720\n\t"
      "s_waitcnt lgkmcnt(6)\n\t"
      "v_mfma_f32_32x32x16_bf16 v[160:175], v[200:203], v[56:59], v[160:175]\n\t"
      "ds_read_b64_tr_b16 v[218:219], v228 offset:31232\n\t"
      "ds_read_b64_tr_b16 v[220:221], v229 offset:30720\n\t"
      "ds_read_b64_tr_b16 v[222:223], v229 offset:31232\n\t"
      "v_mfma_f32_32x32x16_bf16 v[176:191], v[204:207], v[56:59], v[176:191]\n\t"
      "v_add_u32 v224, 4294868992, v224\n\t"
      "v_add_u32 v225, 4294868992, v225\n\t"
      "v_add_u32 v226, 4294868992, v226\n\t"
      "v_add_u32 v227, 4294868992, v227\n\t"
      "s_waitcnt lgkmcnt(4)\n\t"
      "v_mfma_f32_32x32x16_bf16 v[128:143], v[208:211], v[60:63], v[128:143]\n\t"
      "v_add_u32 v228, 4294868992, v228\n\t"
      "v_add_u32 v229, 4294868992, v229\n\t"
      "s_waitcnt lgkmcnt(0)\n\t"
      "s_barrier\n\t"
      "ds_read_b128 v[64:67], v224\n\t"
      "ds_read_b64_tr_b16 v[192:193], v228 offset:16384\n\t"
      "ds_read_b64_tr_b16 v[194:195], v228 offset:16896\n\t"
      "v_mfma_f32_32x32x16_bf16 v[144:159], v[212:215], v[60:63], v[144:159]\n\t"
      "ds_read_b64_tr_b16 v[196:197], v229 offset:16384\n\t"
      "ds_read_b64_tr_b16 v[198:199], v229 offset:16896\n\t"
      "ds_read_b64_tr_b16 v[200:201], v228 offset:24576\n\t"
      "v_mfma_f32_32x32x16_bf16 v[160:175], v[216:219], v[60:63], v[160:175]\n\t"
      "ds_read_b64_tr_b16 v[202:203], v228 offset:25088\n\t"
      "ds_read_b64_tr_b16 v[204:205], v229 offset:24576\n\t"
      "ds_read_b64_tr_b16 v[206:207], v229 offset:25088\n\t"
      "v_mfma_f32_32x32x16_bf16 v[176:191], v[220:223], v[60:63], v[176:191]\n\t"
      "s_waitcnt lgkmcnt(4)\n\t"
      "v_mfma_f32_32x32x16_bf16 v[128:143], v[192:195], v[64:67], v[128:143]\n\t"
      "ds_read_b128 v[68:71], v225\n\t"
      "ds_read_b64_tr_b16 v[208:209], v228 offset:18432\n\t"
      "ds_read_b64_tr_b16 v[210:211], v228 offset:18944\n\t"
      "v_mfma_f32_32x32x16_bf16 v[144:159], v[196:199], v[64:67], v[144:159]\n\t"
      "ds_read_b64_tr_b16 v[212:213], v229 offset:18432\n\t"
      "ds_read_b64_tr_b16 v[214:215], v229 offset:18944\n\t"
      "ds_read_b64_tr_b16 v[216:217], v228 offset:26624\n\t"
      "s_waitcnt lgkmcnt(6)\n\t"
      "v_mfma_f32_32x32x16_bf16 v[160:175], v[200:203], v[64:67], v[160:175]\n\t"
      "ds_read_b64_tr_b16 v[218:219], v228 offset:27136\n\t"
      "ds_read_b64_tr_b16 v[220:221], v229 offset:26624\n\t"
      "ds_read_b64_tr_b16 v[222:223], v229 offset:27136\n\t"
      "v_mfma_f32_32x32x16_bf16 v[176:191], v[204:207], v[64:67], v[176:191]\n\t"
      "s_waitcnt lgkmcnt(4)\n\t"
      "v_mfma_f32_32x32x16_bf16 v[128:143], v[208:211], v[68:71], v[128:143]\n\t"
      "ds_read_b128 v[72:75], v226\n\t"
      "ds_read_b64_tr_b16 v[192:193], v228 offset:20480\n\t"
      "ds_read_b64_tr_b16 v[194:195], v228 offset:20992\n\t"
      "v_mfma_f32_32x32x16_bf16 v[144:159], v[212:215], v[68:71], v[144:159]\n\t"
      "ds_read_b64_tr_b16 v[196:197], v229 offset:20480\n\t"
      "ds_read_b64_tr_b16 v[198:199], v229 offset:20992\n\t"
      "ds_read_b64_tr_b16 v[200:201], v228 offset:28672\n\t"
      "s_waitcnt lgkmcnt(6)\n\t"
      "v_mfma_f32_32x32x16_bf16 v[160:175], v[216:219], v[68:71], v[160:175]\n\t"
      "ds_read_b64_tr_b16 v[202:203], v228 offset:29184\n\t"
      "ds_read_b64_tr_b16 v[204:205], v229 offset:28672\n\t"
      "ds_read_b64_tr_b16 v[206:207], v229 offset:29184\n\t"
      "v_mfma_f32_32x32x16_bf16 v[176:191], v[220:223], v[68:71], v[176:191]\n\t"
      "s_waitcnt lgkmcnt(4)\n\t"
      "v_mfma_f32_32x32x16_bf16 v[128:143], v[192:195], v[72:75], v[128:143]\n\t"
      "ds_read_b128 v[76:79], v227\n\t"
      "ds_read_b64_tr_b16 v[208:209], v228 offset:22528\n\t"
      "ds_read_b64_tr_b16 v[210:211], v228 offset:23040\n\t"
      "v_mfma_f32_32x32x16_bf16 v[144:159], v[196:199], v[72:75], v[144:159]\n\t"
      "ds_read_b64_tr_b16 v[212:213], v229 offset:22528\n\t"
      "ds_read_b64_tr_b16 v[214:215], v229 offset:23040\n\t"
      "ds_read_b64_tr_b16 v[216:217], v228 offset:30720\n\t"
      "s_waitcnt lgkmcnt(6)\n\t"
      "v_mfma_f32_32x32x16_bf16 v[160:175], v[200:203], v[72:75], v[160:175]\n\t"
      "ds_read_b64_tr_b16 v[218:219], v228 offset:31232\n\t"
      "ds_read_b64_tr_b16 v[220:221], v229 offset:30720\n\t"
      "ds_read_b64_tr_b16 v[222:223], v229 offset:31232\n\t"
      "v_mfma_f32_32x32x16_bf16 v[176:191], v[204:207], v[72:75], v[176:191]\n\t"
      "v_add_u32 v224, 32768, v224\n\t"
      "v_add_u32 v225, 32768, v225\n\t"
      "v_add_u32 v226, 32768, v226\n\t"
      "v_add_u32 v227, 32768, v227\n\t"
      "s_waitcnt lgkmcnt(4)\n\t"
      "v_mfma_f32_32x32x16_bf16 v[128:143], v[208:211], v[76:79], v[128:143]\n\t"
      "v_add_u32 v228, 32768, v228\n\t"
      "v_add_u32 v229, 32768, v229\n\t"
      "s_waitcnt lgkmcnt(0)\n\t"
      "s_barrier\n\t"
      "ds_read_b128 v[80:83], v224\n\t"
      "ds_read_b64_tr_b16 v[192:193], v228 offset:16384\n\t"
      "ds_read_b64_tr_b16 v[194:195], v228 offset:16896\n\t"
      "v_mfma_f32_32x32x16_bf16 v[144:159], v[212:215], v[76:79], v[144:159]\n\t"
      "ds_read_b64_tr_b16 v[196:197], v229 offset:16384\n\t"
      "ds_read_b64_tr_b16 v[198:199], v229 offset:16896\n\t"
      "ds_read_b64_tr_b16 v[200:201], v228 offset:24576\n\t"
      "v_mfma_f32_32x32x16_bf16 v[160:175], v[216:219], v[76:79], v[160:175]\n\t"
      "ds_read_b64_tr_b16 v[202:203], v228 offset:25088\n\t"
      "ds_read_b64_tr_b16 v[204:205], v229 offset:24576\n\t"
      "ds_read_b64_tr_b16 v[206:207], v229 offset:25088\n\t"
      "v_mfma_f32_32x32x16_bf16 v[176:191], v[220:223], v[76:79], v[176:191]\n\t"
      "s_waitcnt lgkmcnt(4)\n\t"
      "v_mfma_f32_32x32x16_bf16 v[128:143], v[192:195], v[80:83], v[128:143]\n\t"
      "ds_read_b128 v[84:87], v225\n\t"
      "ds_read_b64_tr_b16 v[208:209], v228 offset:18432\n\t"
      "ds_read_b64_tr_b16 v[210:211], v228 offset:18944\n\t"
      "v_mfma_f32_32x32x16_bf16 v[144:159], v[196:199], v[80:83], v[144:159]\n\t"
      "ds_read_b64_tr_b16 v[212:213], v229 offset:18432\n\t"
      "ds_read_b64_tr_b16 v[214:215], v229 offset:18944\n\t"
      "ds_read_b64_tr_b16 v[216:217], v228 offset:26624\n\t"
      "s_waitcnt lgkmcnt(6)\n\t"
      "v_mfma_f32_32x32x16_bf16 v[160:175], v[200:203], v[80:83], v[160:175]\n\t"
      "ds_read_b64_tr_b16 v[218:219], v228 offset:27136\n\t"
      "ds_read_b64_tr_b16 v[220:221], v229 offset:26624\n\t"
      "ds_read_b64_tr_b16 v[222:223], v229 offset:27136\n\t"
      "v_mfma_f32_32x32x16_bf16 v[176:191], v[204:207], v[80:83], v[176:191]\n\t"
      "s_waitcnt lgkmcnt(4)\n\t"
      "v_mfma_f32_32x32x16_bf16 v[128:143], v[208:211], v[84:87], v[128:143]\n\t"
      "ds_read_b128 v[88:91], v226\n\t"
      "ds_read_b64_tr_b16 v[192:193], v228 offset:20480\n\t"
      "ds_read_b64_tr_b16 v[194:195], v228 offset:20992\n\t"
      "v_mfma_f32_32x32x16_bf16 v[144:159], v[212:215], v[84:87], v[144:159]\n\t"
      "ds_read_b64_tr_b16 v[196:197], v229 offset:20480\n\t"
      "ds_read_b64_tr_b16 v[198:199], v229 offset:20992\n\t"
      "ds_read_b64_tr_b16 v[200:201], v228 offset:28672\n\t"
      "s_waitcnt lgkmcnt(6)\n\t"
      "v_mfma_f32_32x32x16_bf16 v[160:175], v[216:219], v[84:87], v[160:175]\n\t"
      "ds_read_b64_tr_b16 v[202:203], v228 offset:29184\n\t"
      "ds_read_b64_tr_b16 v[204:205], v229 offset:28672\n\t"
      "ds_read_b64_tr_b16 v[206:207], v229 offset:29184\n\t"
      "v_mfma_f32_32x32x16_bf16 v[176:191], v[220:223], v[84:87], v[176:191]\n\t"
      "s_waitcnt lgkmcnt(4)\n\t"
      "v_mfma_f32_32x32x16_bf16 v[128:143], v[192:195], v[88:91], v[128:143]\n\t"
      "ds_read_b128 v[92:95], v227\n\t"
      "ds_read_b64_tr_b16 v[208:209], v228 offset:22528\n\t"
      "ds_read_b64_tr_b16 v[210:211], v228 offset:23040\n\t"
      "v_mfma_f32_32x32x16_bf16 v[144:159], v[196:199], v[88:91], v[144:159]\n\t"
      "ds_read_b64_tr_b16 v[212:213], v229 offset:22528\n\t"
      "ds_read_b64_tr_b16 v[214:215], v229 offset:23040\n\t"
      "ds_read_b64_tr_b16 v[216:217], v228 offset:30720\n\t"
      "s_waitcnt lgkmcnt(6)\n\t"
      "v_mfma_f32_32x32x16_bf16 v[160:175], v[200:203], v[88:91], v[160:175]\n\t"
      "ds_read_b64_tr_b16 v[218:219], v228 offset:31232\n\t"
      "ds_read_b64_tr_b16 v[220:221], v229 offset:30720\n\t"
      "ds_read_b64_tr_b16 v[222:223], v229 offset:31232\n\t"
      "v_mfma_f32_32x32x16_bf16 v[176:191], v[204:207], v[88:91], v[176:191]\n\t"
      "v_add_u32 v224, 32768, v224\n\t"
      "v_add_u32 v225, 32768, v225\n\t"
      "v_add_u32 v226, 32768, v226\n\t"
      "v_add_u32 v227, 32768, v227\n\t"
      "s_waitcnt lgkmcnt(4)\n\t"
      "v_mfma_f32_32x32x16_bf16 v[128:143], v[208:211], v[92:95], v[128:143]\n\t"
      "v_add_u32 v228, 32768, v228\n\t"
      "v_add_u32 v229, 32768, v229\n\t"
      "s_waitcnt lgkmcnt(0)\n\t"
      "s_barrier\n\t"
      "ds_read_b128 v[96:99], v224\n\t"
      "ds_read_b64_tr_b16 v[192:193], v228 offset:16384\n\t"
      "ds_read_b64_tr_b16 v[194:195], v228 offset:16896\n\t"
      "v_mfma_f32_32x32x16_bf16 v[144:159], v[212:215], v[92:95], v[144:159]\n\t"
      "ds_read_b64_tr_b16 v[196:197], v229 offset:16384\n\t"
      "ds_read_b64_tr_b16 v[198:199], v229 offset:16896\n\t"
      "ds_read_b64_tr_b16 v[200:201], v228 offset:24576\n\t"
      "v_mfma_f32_32x32x16_bf16 v[160:175], v[216:219], v[92:95], v[160:175]\n\t"
      "ds_read_b64_tr_b16 v[202:203], v228 offset:25088\n\t"
      "ds_read_b64_tr_b16 v[204:205], v229 offset:24576\n\t"
      "ds_read_b64_tr_b16 v[206:207], v229 offset:25088\n\t"
      "v_mfma_f32_32x32x16_bf16 v[176:191], v[220:223], v[92:95], v[176:191]\n\t"
      "s_waitcnt lgkmcnt(4)\n\t"
      "v_mfma_f32_32x32x16_bf16 v[128:143], v[192:195], v[96:99], v[128:143]\n\t"
      "ds_read_b128 v[100:103], v225\n\t"
      "ds_read_b64_tr_b16 v[208:209], v228 offset:18432\n\t"
      "ds_read_b64_tr_b16 v[210:211], v228 offset:18944\n\t"
      "v_mfma_f32_32x32x16_bf16 v[144:159], v[196:199], v[96:99], v[144:159]\n\t"
      "ds_read_b64_tr_b16 v[212:213], v229 offset:18432\n\t"
      "ds_read_b64_tr_b16 v[214:215], v229 offset:18944\n\t"
      "ds_read_b64_tr_b16 v[216:217], v228 offset:26624\n\t"
      "s_waitcnt lgkmcnt(6)\n\t"
      "v_mfma_f32_32x32x16_bf16 v[160:175], v[200:203], v[96:99], v[160:175]\n\t"
      "ds_read_b64_tr_b16 v[218:219], v228 offset:27136\n\t"
      "ds_read_b64_tr_b16 v[220:221], v229 offset:26624\n\t"
      "ds_read_b64_tr_b16 v[222:223], v229 offset:27136\n\t"
      "v_mfma_f32_32x32x16_bf16 v[176:191], v[204:207], v[96:99], v[176:191]\n\t"
      "s_waitcnt lgkmcnt(4)\n\t"
      "v_mfma_f32_32x32x16_bf16 v[128:143], v[208:211], v[100:103], v[128:143]\n\t"
      "ds_read_b128 v[104:107], v226\n\t"
      "ds_read_b64_tr_b16 v[192:193], v228 offset:20480\n\t"
      "ds_read_b64_tr_b16 v[194:195], v228 offset:20992\n\t"
      "v_mfma_f32_32x32x16_bf16 v[144:159], v[212:215], v[100:103], v[144:159]\n\t"
      "ds_read_b64_tr_b16 v[196:197], v229 offset:20480\n\t"
      "ds_read_b64_tr_b16 v[198:199], v229 offset:20992\n\t"
      "ds_read_b64_tr_b16 v[200:201], v228 offset:28672\n\t"
      "s_waitcnt lgkmcnt(6)\n\t"
      "v_mfma_f32_32x32x16_bf16 v[160:175], v[216:219], v[100:103], v[160:175]\n\t"
      "ds_read_b64_tr_b16 v[202:203], v228 offset:29184\n\t"
      "ds_read_b64_tr_b16 v[204:205], v229 offset:28672\n\t"
      "ds_read_b64_tr_b16 v[206:207], v229 offset:29184\n\t"
      "v_mfma_f32_32x32x16_bf16 v[176:191], v[220:223], v[100:103], v[176:191]\n\t"
      "s_waitcnt lgkmcnt(4)\n\t"
      "v_mfma_f32_32x32x16_bf16 v[128:143], v[192:195], v[104:107], v[128:143]\n\t"
      "ds_read_b128 v[108:111], v227\n\t"
      "ds_read_b64_tr_b16 v[208:209], v228 offset:22528\n\t"
      "ds_read_b64_tr_b16 v[210:211], v228 offset:23040\n\t"
      "v_mfma_f32_32x32x16_bf16 v[144:159], v[196:199], v[104:107], v[144:159]\n\t"
      "ds_read_b64_tr_b16 v[212:213], v229 offset:22528\n\t"
      "ds_read_b64_tr_b16 v[214:215], v229 offset:23040\n\t"
      "ds_read_b64_tr_b16 v[216:217], v228 offset:30720\n\t"
      "s_waitcnt lgkmcnt(6)\n\t"
      "v_mfma_f32_32x32x16_bf16 v[160:175], v[200:203], v[104:107], v[160:175]\n\t"
      "ds_read_b64_tr_b16 v[218:219], v228 offset:31232\n\t"
      "ds_read_b64_tr_b16 v[220:221], v229 offset:30720\n\t"
      "ds_read_b64_tr_b16 v[222:223], v229 offset:31232\n\t"
      "v_mfma_f32_32x32x16_bf16 v[176:191], v[204:207], v[104:107], v[176:191]\n\t"
      "v_add_u32 v224, 32768, v224\n\t"
      "v_add_u32 v225, 32768, v225\n\t"
      "v_add_u32 v226, 32768, v226\n\t"
      "v_add_u32 v227, 32768, v227\n\t"
      "s_waitcnt lgkmcnt(4)\n\t"
      "v_mfma_f32_32x32x16_bf16 v[128:143], v[208:211], v[108:111], v[128:143]\n\t"
      "v_add_u32 v228, 32768, v228\n\t"
      "v_add_u32 v229, 32768, v229\n\t"
      "s_waitcnt lgkmcnt(0)\n\t"
      "s_barrier\n\t"
      "ds_read_b128 v[112:115], v224\n\t"
      "ds_read_b64_tr_b16 v[192:193], v228 offset:16384\n\t"
      "ds_read_b64_tr_b16 v[194:195], v228 offset:16896\n\t"
      "v_mfma_f32_32x32x16_bf16 v[144:159], v[212:215], v[108:111], v[144:159]\n\t"
      "ds_read_b64_tr_b16 v[196:197], v229 offset:16384\n\t"
      "ds_read_b64_tr_b16 v[198:199], v229 offset:16896\n\t"
      "ds_read_b64_tr_b16 v[200:201], v228 offset:24576\n\t"
      "v_mfma_f32_32x32x16_bf16 v[160:175], v[216:219], v[108:111], v[160:175]\n\t"
      "ds_read_b64_tr_b16 v[202:203], v228 offset:25088\n\t"
      "ds_read_b64_tr_b16 v[204:205], v229 offset:24576\n\t"
      "ds_read_b64_tr_b16 v[206:207], v229 offset:25088\n\t"
      "v_mfma_f32_32x32x16_bf16 v[176:191], v[220:223], v[108:111], v[176:191]\n\t"
      "s_waitcnt lgkmcnt(4)\n\t"
      "v_mfma_f32_32x32x16_bf16 v[128:143], v[192:195], v[112:115], v[128:143]\n\t"
      "ds_read_b128 v[116:119], v225\n\t"
      "ds_read_b64_tr_b16 v[208:209], v228 offset:18432\n\t"
      "ds_read_b64_tr_b16 v[210:211], v228 offset:18944\n\t"
      "v_mfma_f32_32x32x16_bf16 v[144:159], v[196:199], v[112:115], v[144:159]\n\t"
      "ds_read_b64_tr_b16 v[212:213], v229 offset:18432\n\t"
      "ds_read_b64_tr_b16 v[214:215], v229 offset:18944\n\t"
      "ds_read_b64_tr_b16 v[216:217], v228 offset:26624\n\t"
      "s_waitcnt lgkmcnt(6)\n\t"
      "v_mfma_f32_32x32x16_bf16 v[160:175], v[200:203], v[112:115], v[160:175]\n\t"
      "ds_read_b64_tr_b16 v[218:219], v228 offset:27136\n\t"
      "ds_read_b64_tr_b16 v[220:221], v229 offset:26624\n\t"
      "ds_read_b64_tr_b16 v[222:223], v229 offset:27136\n\t"
      "v_mfma_f32_32x32x16_bf16 v[176:191], v[204:207], v[112:115], v[176:191]\n\t"
      "s_waitcnt lgkmcnt(4)\n\t"
      "v_mfma_f32_32x32x16_bf16 v[128:143], v[208:211], v[116:119], v[128:143]\n\t"
      "ds_read_b128 v[120:123], v226\n\t"
      "ds_read_b64_tr_b16 v[192:193], v228 offset:20480\n\t"
      "ds_read_b64_tr_b16 v[194:195], v228 offset:20992\n\t"
      "v_mfma_f32_32x32x16_bf16 v[144:159], v[212:215], v[116:119], v[144:159]\n\t"
      "ds_read_b64_tr_b16 v[196:197], v229 offset:20480\n\t"
      "ds_read_b64_tr_b16 v[198:199], v229 offset:20992\n\t"
      "ds_read_b64_tr_b16 v[200:201], v228 offset:28672\n\t"
      "s_waitcnt lgkmcnt(6)\n\t"
      "v_mfma_f32_32x32x16_bf16 v[160:175], v[216:219], v[116:119], v[160:175]\n\t"
      "ds_read_b64_tr_b16 v[202:203], v228 offset:29184\n\t"
      "ds_read_b64_tr_b16 v[204:205], v229 offset:28672\n\t"
      "ds_read_b64_tr_b16 v[206:207], v229 offset:29184\n\t"
      "v_mfma_f32_32x32x16_bf16 v[176:191], v[220:223], v[116:119], v[176:191]\n\t"
      "s_waitcnt lgkmcnt(4)\n\t"
      "v_mfma_f32_32x32x16_bf16 v[128:143], v[192:195], v[120:123], v[128:143]\n\t"
      "ds_read_b128 v[124:127], v227\n\t"
      "ds_read_b64_tr_b16 v[208:209], v228 offset:22528\n\t"
      "ds_read_b64_tr_b16 v[210:211], v228 offset:23040\n\t"
      "v_mfma_f32_32x32x16_bf16 v[144:159], v[196:199], v[120:123], v[144:159]\n\t"
      "ds_read_b64_tr_b16 v[212:213], v229 offset:22528\n\t"
      "ds_read_b64_tr_b16 v[214:215], v229 offset:23040\n\t"
      "ds_read_b64_tr_b16 v[216:217], v228 offset:30720\n\t"
      "s_waitcnt lgkmcnt(6)\n\t"
      "v_mfma_f32_32x32x16_bf16 v[160:175], v[200:203], v[120:123], v[160:175]\n\t"
      "ds_read_b64_tr_b16 v[218:219], v228 offset:31232\n\t"
      "ds_read_b64_tr_b16 v[220:221], v229 offset:30720\n\t"
      "ds_read_b64_tr_b16 v[222:223], v229 offset:31232\n\t"
      "v_mfma_f32_32x32x16_bf16 v[176:191], v[204:207], v[120:123], v[176:191]\n\t"
      "s_waitcnt lgkmcnt(4)\n\t"
      "v_mfma_f32_32x32x16_bf16 v[128:143], v[208:211], v[124:127], v[128:143]\n\t"
      "s_waitcnt lgkmcnt(0)\n\t"
      "s_barrier\n\t"
      "v_mfma_f32_32x32x16_bf16 v[144:159], v[212:215], v[124:127], v[144:159]\n\t"
      "v_mfma_f32_32x32x16_bf16 v[160:175], v[216:219], v[124:127], v[160:175]\n\t"
      "v_mfma_f32_32x32x16_bf16 v[176:191], v[220:223], v[124:127], v[176:191]\n\t"
      "s_nop 15\n\t"
          P1OPS);
      const unsigned dq = WS_QF + hb + lane_now() * 16;
#pragma unroll
      for (int bk = 0; bk < 2; bk++) {
        WST(pk8(acc[bk], 0, 0.25f * LOG2E), dq + bk * 131072);
        WST(pk8(acc[bk], 8, 0.25f * LOG2E), dq + bk * 131072 + 65536);
      }
      wa = (u32x4){wa[2], wa[2] ^ 64, 0, 0};
      asm volatile(
      "ds_read_b64_tr_b16 v[192:193], v228 offset:0\n\t"
      "ds_read_b64_tr_b16 v[194:195], v228 offset:512\n\t"
      "ds_read_b64_tr_b16 v[196:197], v228 offset:2048\n\t"
      "ds_read_b64_tr_b16 v[198:199], v228 offset:2560\n\t"
      "ds_read_b64_tr_b16 v[200:201], v228 offset:4096\n\t"
      "ds_read_b64_tr_b16 v[202:203], v228 offset:4608\n\t"
      "ds_read_b64_tr_b16 v[204:205], v228 offset:6144\n\t"
      "ds_read_b64_tr_b16 v[206:207], v228 offset:6656\n\t"
      "ds_read_b64_tr_b16 v[208:209], v228 offset:8192\n\t"
      "ds_read_b64_tr_b16 v[210:211], v228 offset:8704\n\t"
      "ds_read_b64_tr_b16 v[212:213], v228 offset:10240\n\t"
      "ds_read_b64_tr_b16 v[214:215], v228 offset:10752\n\t"
      "s_waitcnt lgkmcnt(8)\n\t"
      "v_mfma_f32_32x32x16_bf16 v[128:143], v[192:195], v[0:3], 0\n\t"
      "ds_read_b64_tr_b16 v[216:217], v228 offset:12288\n\t"
      "ds_read_b64_tr_b16 v[218:219], v228 offset:12800\n\t"
      "v_mul_f32 v224, %[nl], v160\n\t"
      "v_mul_f32 v225, %[nl], v161\n\t"
      "v_exp_f32 v224, v224\n\t"
      "v_exp_f32 v225, v225\n\t"
      "v_mul_f32 v226, %[nl], v162\n\t"
      "v_mfma_f32_32x32x16_bf16 v[232:247], v[196:199], v[4:7], 0\n\t"
      "ds_read_b64_tr_b16 v[220:221], v228 offset:14336\n\t"
      "ds_read_b64_tr_b16 v[222:223], v228 offset:14848\n\t"
      "v_mul_f32 v227, %[nl], v163\n\t"
      "v_exp_f32 v226, v226\n\t"
      "v_exp_f32 v227, v227\n\t"
      "v_add_f32 v224, 1.0, v224\n\t"
      "v_add_f32 v225, 1.0, v225\n\t"
      "s_waitcnt lgkmcnt(8)\n\t"
      "v_mfma_f32_32x32x16_bf16 v[128:143], v[200:203], v[8:11], v[128:143]\n\t"
      "ds_read_b64_tr_b16 v[192:193], v228 offset:16384\n\t"
      "ds_read_b64_tr_b16 v[194:195], v228 offset:16896\n\t"
      "v_rcp_f32 v224, v224\n\t"
      "v_rcp_f32 v225, v225\n\t"
      "v_add_f32 v226, 1.0, v226\n\t"
      "v_add_f32 v227, 1.0, v227\n\t"
      "v_mfma_f32_32x32x16_bf16 v[232:247], v[204:207], v[12:15], v[232:247]\n\t"
      "ds_read_b64_tr_b16 v[196:197], v228 offset:18432\n\t"
      "ds_read_b64_tr_b16 v[198:199], v228 offset:18944\n\t"
      "v_rcp_f32 v226, v226\n\t"
      "v_rcp_f32 v227, v227\n\t"
      "v_mul_f32 v224, v160, v224\n\t"
      "v_mul_f32 v225, v161, v225\n\t"
      "v_cvt_pk_bf16_f32 v160, v224, v225\n\t"
      "v_mul_f32 v226, v162, v226\n\t"
      "s_waitcnt lgkmcnt(8)\n\t"
      "v_mfma_f32_32x32x16_bf16 v[128:143], v[208:211], v[16:19], v[128:143]\n\t"
      "ds_read_b64_tr_b16 v[200:201], v228 offset:20480\n\t"
      "ds_read_b64_tr_b16 v[202:203], v228 offset:20992\n\t"
      "v_mul_f32 v227, v163, v227\n\t"
      "v_cvt_pk_bf16_f32 v161, v226, v227\n\t"
      "v_mul_f32 v224, %[nl], v164\n\t"
      "v_mul_f32 v225, %[nl], v165\n\t"
      "v_exp_f32 v224, v224\n\t"
      "v_mfma_f32_32x32x16_bf16 v[232:247], v[212:215], v[20:23], v[232:247]\n\t"
      "ds_read_b64_tr_b16 v[204:205], v228 offset:22528\n\t"
      "ds_read_b64_tr_b16 v[206:207], v228 offset:23040\n\t"
      "v_exp_f32 v225, v225\n\t"
      "v_mul_f32 v226, %[nl], v166\n\t"
      "v_mul_f32 v227, %[nl], v167\n\t"
      "v_exp_f32 v226, v226\n\t"
      "v_exp_f32 v227, v227\n\t"
      "s_waitcnt lgkmcnt(8)\n\t"
      "v_mfma_f32_32x32x16_bf16 v[128:143], v[216:219], v[24:27], v[128:143]\n\t"
      "ds_read_b64_tr_b16 v[208:209], v228 offset:24576\n\t"
      "ds_read_b64_tr_b16 v[210:211], v228 offset:25088\n\t"
      "v_add_f32 v224, 1.0, v224\n\t"
      "v_add_f32 v225, 1.0, v225\n\t"
      "v_rcp_f32 v224, v224\n\t"
      "v_rcp_f32 v225, v225\n\t"
      "v_add_f32 v226, 1.0, v226\n\t"
      "v_mfma_f32_32x32x16_bf16 v[232:247], v[220:223], v[28:31], v[232:247]\n\t"
      "ds_read_b64_tr_b16 v[212:213], v228 offset:26624\n\t"
      "ds_read_b64_tr_b16 v[214:215], v228 offset:27136\n\t"
      "v_add_f32 v227, 1.0, v227\n\t"
      "v_rcp_f32 v226, v226\n\t"
      "v_rcp_f32 v227, v227\n\t"
      "v_mul_f32 v224, v164, v224\n\t"
      "v_mul_f32 v225, v165, v225\n\t"
      "s_waitcnt lgkmcnt(8)\n\t"
      "v_mfma_f32_32x32x16_bf16 v[128:143], v[192:195], v[32:35], v[128:143]\n\t"
      "ds_read_b64_tr_b16 v[216:217], v228 offset:28672\n\t"
      "ds_read_b64_tr_b16 v[218:219], v228 offset:29184\n\t"
      "v_cvt_pk_bf16_f32 v162, v224, v225\n\t"
      "v_mul_f32 v226, v166, v226\n\t"
      "v_mul_f32 v227, v167, v227\n\t"
      "v_cvt_pk_bf16_f32 v163, v226, v227\n\t"
      "v_mul_f32 v224, %[nl], v168\n\t"
      "v_mul_f32 v225, %[nl], v169\n\t"
      "v_mfma_f32_32x32x16_bf16 v[232:247], v[196:199], v[36:39], v[232:247]\n\t"
      "ds_read_b64_tr_b16 v[220:221], v228 offset:30720\n\t"
      "ds_read_b64_tr_b16 v[222:223], v228 offset:31232\n\t"
      "v_exp_f32 v224, v224\n\t"
      "v_exp_f32 v225, v225\n\t"
      "v_mul_f32 v226, %[nl], v170\n\t"
      "v_mul_f32 v227, %[nl], v171\n\t"
      "v_exp_f32 v226, v226\n\t"
      "s_waitcnt lgkmcnt(8)\n\t"
      "v_mfma_f32_32x32x16_bf16 v[128:143], v[200:203], v[40:43], v[128:143]\n\t"
      "s_barrier\n\t"
      "ds_read_b64_tr_b16 v[192:193], v228 offset:32768\n\t"
      "ds_read_b64_tr_b16 v[194:195], v228 offset:33280\n\t"
      "v_exp_f32 v227, v227\n\t"
      "v_add_f32 v224, 1.0, v224\n\t"
      "v_add_f32 v225, 1.0, v225\n\t"
      "v_rcp_f32 v224, v224\n\t"
      "v_mfma_f32_32x32x16_bf16 v[232:247], v[204:207], v[44:47], v[232:247]\n\t"
      "ds_read_b64_tr_b16 v[196:197], v228 offset:34816\n\t"
      "ds_read_b64_tr_b16 v[198:199], v228 offset:35328\n\t"
      "v_rcp_f32 v225, v225\n\t"
      "v_add_f32 v226, 1.0, v226\n\t"
      "v_add_f32 v227, 1.0, v227\n\t"
      "v_rcp_f32 v226, v226\n\t"
      "v_rcp_f32 v227, v227\n\t"
      "s_waitcnt lgkmcnt(8)\n\t"
      "v_mfma_f32_32x32x16_bf16 v[128:143], v[208:211], v[48:51], v[128:143]\n\t"
      "ds_read_b64_tr_b16 v[200:201], v228 offset:36864\n\t"
      "ds_read_b64_tr_b16 v[202:203], v228 offset:37376\n\t"
      "v_mul_f32 v224, v168, v224\n\t"
      "v_mul_f32 v225, v169, v225\n\t"
      "v_cvt_pk_bf16_f32 v164, v224, v225\n\t"
      "v_mul_f32 v226, v170, v226\n\t"
      "v_mul_f32 v227, v171, v227\n\t"
      "v_cvt_pk_bf16_f32 v165, v226, v227\n\t"
      "v_mul_f32 v224, %[nl], v172\n\t"
      "v_mfma_f32_32x32x16_bf16 v[232:247], v[212:215], v[52:55], v[232:247]\n\t"
      "ds_read_b64_tr_b16 v[204:205], v228 offset:38912\n\t"
      "ds_read_b64_tr_b16 v[206:207], v228 offset:39424\n\t"
      "v_mul_f32 v225, %[nl], v173\n\t"
      "v_exp_f32 v224, v224\n\t"
      "v_exp_f32 v225, v225\n\t"
      "v_mul_f32 v226, %[nl], v174\n\t"
      "v_mul_f32 v227, %[nl], v175\n\t"
      "s_waitcnt lgkmcnt(8)\n\t"
      "v_mfma_f32_32x32x16_bf16 v[128:143], v[216:219], v[56:59], v[128:143]\n\t"
      "ds_read_b64_tr_b16 v[208:209], v228 offset:40960\n\t"
      "ds_read_b64_tr_b16 v[210:211], v228 offset:41472\n\t"
      "v_exp_f32 v226, v226\n\t"
      "v_exp_f32 v227, v227\n\t"
      "v_add_f32 v224, 1.0, v224\n\t"
      "v_add_f32 v225, 1.0, v225\n\t"
      "v_mfma_f32_32x32x16_bf16 v[232:247], v[220:223], v[60:63], v[232:247]\n\t"
      "ds_read_b64_tr_b16 v[212:213], v228 offset:43008\n\t"
      "ds_read_b64_tr_b16 v[214:215], v228 offset:43520\n\t"
      "v_rcp_f32 v224, v224\n\t"
      "v_rcp_f32 v225, v225\n\t"
      "v_add_f32 v226, 1.0, v226\n\t"
      "v_add_f32 v227, 1.0, v227\n\t"
      "v_rcp_f32 v226, v226\n\t"
      "s_waitcnt lgkmcnt(8)\n\t"
      "v_mfma_f32_32x32x16_bf16 v[128:143], v[192:195], v[64:67], v[128:143]\n\t"
      "ds_read_b64_tr_b16 v[216:217], v228 offset:45056\n\t"
      "ds_read_b64_tr_b16 v[218:219], v228 offset:45568\n\t"
      "v_rcp_f32 v227, v227\n\t"
      "v_mul_f32 v224, v172, v224\n\t"
      "v_mul_f32 v225, v173, v225\n\t"
      "v_cvt_pk_bf16_f32 v166, v224, v225\n\t"
      "v_mul_f32 v226, v174, v226\n\t"
      "v_mul_f32 v227, v175, v227\n\t"
      "v_mfma_f32_32x32x16_bf16 v[232:247], v[196:199], v[68:71], v[232:247]\n\t"
      "ds_read_b64_tr_b16 v[220:221], v228 offset:47104\n\t"
      "ds_read_b64_tr_b16 v[222:223], v228 offset:47616\n\t"
      "v_cvt_pk_bf16_f32 v167, v226, v227\n\t"
      "v_mul_f32 v224, %[nl], v176\n\t"
      "v_mul_f32 v225, %[nl], v177\n\t"
      "v_exp_f32 v224, v224\n\t"
      "v_exp_f32 v225, v225\n\t"
      "s_waitcnt lgkmcnt(8)\n\t"
      "v_mfma_f32_32x32x16_bf16 v[128:143], v[200:203], v[72:75], v[128:143]\n\t"
      "ds_read_b64_tr_b16 v[192:193], v228 offset:49152\n\t"
      "ds_read_b64_tr_b16 v[194:195], v228 offset:49664\n\t"
      "v_mul_f32 v226, %[nl], v178\n\t"
      "v_mul_f32 v227, %[nl], v179\n\t"
      "v_exp_f32 v226, v226\n\t"
      "v_exp_f32 v227, v227\n\t"
      "v_add_f32 v224, 1.0, v224\n\t"
      "v_mfma_f32_32x32x16_bf16 v[232:247], v[204:207], v[76:79], v[232:247]\n\t"
      "ds_read_b64_tr_b16 v[196:197], v228 offset:51200\n\t"
      "ds_read_b64_tr_b16 v[198:199], v228 offset:51712\n\t"
      "v_add_f32 v225, 1.0, v225\n\t"
      "v_rcp_f32 v224, v224\n\t"
      "v_rcp_f32 v225, v225\n\t"
      "v_add_f32 v226, 1.0, v226\n\t"
      "v_add_f32 v227, 1.0, v227\n\t"
      "s_waitcnt lgkmcnt(8)\n\t"
      "v_mfma_f32_32x32x16_bf16 v[128:143], v[208:211], v[80:83], v[128:143]\n\t"
      "ds_read_b64_tr_b16 v[200:201], v228 offset:53248\n\t"
      "ds_read_b64_tr_b16 v[202:203], v228 offset:53760\n\t"
      "v_rcp_f32 v226, v226\n\t"
      "v_rcp_f32 v227, v227\n\t"
      "v_mul_f32 v224, v176, v224\n\t"
      "v_mul_f32 v225, v177, v225\n\t"
      "v_cvt_pk_bf16_f32 v168, v224, v225\n\t"
      "v_mfma_f32_32x32x16_bf16 v[232:247], v[212:215], v[84:87], v[232:247]\n\t"
      "ds_read_b64_tr_b16 v[204:205], v228 offset:55296\n\t"
      "ds_read_b64_tr_b16 v[206:207], v228 offset:55808\n\t"
      "v_mul_f32 v226, v178, v226\n\t"
      "v_mul_f32 v227, v179, v227\n\t"
      "v_cvt_pk_bf16_f32 v169, v226, v227\n\t"
      "v_mul_f32 v224, %[nl], v180\n\t"
      "v_mul_f32 v225, %[nl], v181\n\t"
      "v_exp_f32 v224, v224\n\t"
      "s_waitcnt lgkmcnt(8)\n\t"
      "v_mfma_f32_32x32x16_bf16 v[128:143], v[216:219], v[88:91], v[128:143]\n\t"
      "ds_read_b64_tr_b16 v[208:209], v228 offset:57344\n\t"
      "ds_read_b64_tr_b16 v[210:211], v228 offset:57856\n\t"
      "v_exp_f32 v225, v225\n\t"
      "v_mul_f32 v226, %[nl], v182\n\t"
      "v_mul_f32 v227, %[nl], v183\n\t"
      "v_exp_f32 v226, v226\n\t"
      "v_mfma_f32_32x32x16_bf16 v[232:247], v[220:223], v[92:95], v[232:247]\n\t"
      "ds_read_b64_tr_b16 v[212:213], v228 offset:59392\n\t"
      "ds_read_b64_tr_b16 v[214:215], v228 offset:59904\n\t"
      "v_exp_f32 v227, v227\n\t"
      "v_add_f32 v224, 1.0, v224\n\t"
      "v_add_f32 v225, 1.0, v225\n\t"
      "v_rcp_f32 v224, v224\n\t"
      "v_rcp_f32 v225, v225\n\t"
      "s_waitcnt lgkmcnt(8)\n\t"
      "v_mfma_f32_32x32x16_bf16 v[128:143], v[192:195], v[96:99], v[128:143]\n\t"
      "ds_read_b64_tr_b16 v[216:217], v228 offset:61440\n\t"
      "ds_read_b64_tr_b16 v[218:219], v228 offset:61952\n\t"
      "v_add_f32 v226, 1.0, v226\n\t"
      "v_add_f32 v227, 1.0, v227\n\t"
      "v_rcp_f32 v226, v226\n\t"
      "v_rcp_f32 v227, v227\n\t"
      "v_mul_f32 v224, v180, v224\n\t"
      "v_mfma_f32_32x32x16_bf16 v[232:247], v[196:199], v[100:103], v[232:247]\n\t"
      "ds_read_b64_tr_b16 v[220:221], v228 offset:63488\n\t"
      "ds_read_b64_tr_b16 v[222:223], v228 offset:64000\n\t"
      "v_mul_f32 v225, v181, v225\n\t"
      "v_cvt_pk_bf16_f32 v170, v224, v225\n\t"
      "v_mul_f32 v226, v182, v226\n\t"
      "v_mul_f32 v227, v183, v227\n\t"
      "v_cvt_pk_bf16_f32 v171, v226, v227\n\t"
      "v_mul_f32 v224, %[nl], v184\n\t"
      "v_mul_f32 v225, %[nl], v185\n\t"
      "s_waitcnt lgkmcnt(8)\n\t"
      "v_mfma_f32_32x32x16_bf16 v[128:143], v[200:203], v[104:107], v[128:143]\n\t"
      "ds_read_b64_tr_b16 v[192:193], v229 offset:0\n\t"
      "ds_read_b64_tr_b16 v[194:195], v229 offset:512\n\t"
      "v_exp_f32 v224, v224\n\t"
      "v_exp_f32 v225, v225\n\t"
      "v_mul_f32 v226, %[nl], v186\n\t"
      "v_mul_f32 v227, %[nl], v187\n\t"
      "v_mfma_f32_32x32x16_bf16 v[232:247], v[204:207], v[108:111], v[232:247]\n\t"
      "ds_read_b64_tr_b16 v[196:197], v229 offset:2048\n\t"
      "ds_read_b64_tr_b16 v[198:199], v229 offset:2560\n\t"
      "v_exp_f32 v226, v226\n\t"
      "v_exp_f32 v227, v227\n\t"
      "v_add_f32 v224, 1.0, v224\n\t"
      "v_add_f32 v225, 1.0, v225\n\t"
      "v_rcp_f32 v224, v224\n\t"
      "s_waitcnt lgkmcnt(8)\n\t"
      "v_mfma_f32_32x32x16_bf16 v[128:143], v[208:211], v[112:115], v[128:143]\n\t"
      "ds_read_b64_tr_b16 v[200:201], v229 offset:4096\n\t"
      "ds_read_b64_tr_b16 v[202:203], v229 offset:4608\n\t"
      "v_rcp_f32 v225, v225\n\t"
      "v_add_f32 v226, 1.0, v226\n\t"
      "v_add_f32 v227, 1.0, v227\n\t"
      "v_rcp_f32 v226, v226\n\t"
      "v_mfma_f32_32x32x16_bf16 v[232:247], v[212:215], v[116:119], v[232:247]\n\t"
      "ds_read_b64_tr_b16 v[204:205], v229 offset:6144\n\t"
      "ds_read_b64_tr_b16 v[206:207], v229 offset:6656\n\t"
      "v_rcp_f32 v227, v227\n\t"
      "v_mul_f32 v224, v184, v224\n\t"
      "v_mul_f32 v225, v185, v225\n\t"
      "v_cvt_pk_bf16_f32 v172, v224, v225\n\t"
      "v_mul_f32 v226, v186, v226\n\t"
      "v_mul_f32 v227, v187, v227\n\t"
      "v_cvt_pk_bf16_f32 v173, v226, v227\n\t"
      "s_waitcnt lgkmcnt(8)\n\t"
      "v_mfma_f32_32x32x16_bf16 v[128:143], v[216:219], v[120:123], v[128:143]\n\t"
      "ds_read_b64_tr_b16 v[208:209], v229 offset:8192\n\t"
      "ds_read_b64_tr_b16 v[210:211], v229 offset:8704\n\t"
      "v_mul_f32 v224, %[nl], v188\n\t"
      "v_mul_f32 v225, %[nl], v189\n\t"
      "v_exp_f32 v224, v224\n\t"
      "v_exp_f32 v225, v225\n\t"
      "v_mul_f32 v226, %[nl], v190\n\t"
      "v_mfma_f32_32x32x16_bf16 v[232:247], v[220:223], v[124:127], v[232:247]\n\t"
      "ds_read_b64_tr_b16 v[212:213], v229 offset:10240\n\t"
      "ds_read_b64_tr_b16 v[214:215], v229 offset:10752\n\t"
      "v_mul_f32 v227, %[nl], v191\n\t"
      "v_exp_f32 v226, v226\n\t"
      "v_exp_f32 v227, v227\n\t"
      "v_add_f32 v224, 1.0, v224\n\t"
      "v_add_f32 v225, 1.0, v225\n\t"
      "s_waitcnt lgkmcnt(8)\n\t"
      "v_mfma_f32_32x32x16_bf16 v[144:159], v[192:195], v[0:3], 0\n\t"
      "ds_read_b64_tr_b16 v[216:217], v229 offset:12288\n\t"
      "ds_read_b64_tr_b16 v[218:219], v229 offset:12800\n\t"
      "v_rcp_f32 v224, v224\n\t"
      "v_rcp_f32 v225, v225\n\t"
      "v_add_f32 v226, 1.0, v226\n\t"
      "v_add_f32 v227, 1.0, v227\n\t"
      "v_rcp_f32 v226, v226\n\t"
      "v_rcp_f32 v227, v227\n\t"
      "v_mul_f32 v224, v188, v224\n\t"
      "v_mul_f32 v225, v189, v225\n\t"
      "v_cvt_pk_bf16_f32 v174, v224, v225\n\t"
      "v_mul_f32 v226, v190, v226\n\t"
      "v_mul_f32 v227, v191, v227\n\t"
      "v_mfma_f32_32x32x16_bf16 v[176:191], v[196:199], v[4:7], 0\n\t"
      "ds_read_b64_tr_b16 v[220:221], v229 offset:14336\n\t"
      "ds_read_b64_tr_b16 v[222:223], v229 offset:14848\n\t"
      "v_cvt_pk_bf16_f32 v175, v226, v227\n\t"
      "s_waitcnt lgkmcnt(8)\n\t"
      "v_mfma_f32_32x32x16_bf16 v[144:159], v[200:203], v[8:11], v[144:159]\n\t"
      "ds_read_b64_tr_b16 v[192:193], v229 offset:16384\n\t"
      "ds_read_b64_tr_b16 v[194:195], v229 offset:16896\n\t"
      "v_mfma_f32_32x32x16_bf16 v[176:191], v[204:207], v[12:15], v[176:191]\n\t"
      "ds_read_b64_tr_b16 v[196:197], v229 offset:18432\n\t"
      "ds_read_b64_tr_b16 v[198:199], v229 offset:18944\n\t"
      "s_waitcnt lgkmcnt(8)\n\t"
      "v_mfma_f32_32x32x16_bf16 v[144:159], v[208:211], v[16:19], v[144:159]\n\t"
      "ds_read_b64_tr_b16 v[200:201], v229 offset:20480\n\t"
      "ds_read_b64_tr_b16 v[202:203], v229 offset:20992\n\t"
      "v_add_f32 v128, v128, v232\n\t"
      "v_add_f32 v129, v129, v233\n\t"
      "v_mul_f32 v224, %[nl], v128\n\t"
      "v_mul_f32 v225, %[nl], v129\n\t"
      "v_exp_f32 v224, v224\n\t"
      "v_exp_f32 v225, v225\n\t"
      "v_add_f32 v130, v130, v234\n\t"
      "v_mfma_f32_32x32x16_bf16 v[176:191], v[212:215], v[20:23], v[176:191]\n\t"
      "ds_read_b64_tr_b16 v[204:205], v229 offset:22528\n\t"
      "ds_read_b64_tr_b16 v[206:207], v229 offset:23040\n\t"
      "v_add_f32 v131, v131, v235\n\t"
      "v_mul_f32 v226, %[nl], v130\n\t"
      "v_mul_f32 v227, %[nl], v131\n\t"
      "v_exp_f32 v226, v226\n\t"
      "v_exp_f32 v227, v227\n\t"
      "s_waitcnt lgkmcnt(8)\n\t"
      "v_mfma_f32_32x32x16_bf16 v[144:159], v[216:219], v[24:27], v[144:159]\n\t"
      "ds_read_b64_tr_b16 v[208:209], v229 offset:24576\n\t"
      "ds_read_b64_tr_b16 v[210:211], v229 offset:25088\n\t"
      "v_add_f32 v224, 1.0, v224\n\t"
      "v_add_f32 v225, 1.0, v225\n\t"
      "v_rcp_f32 v224, v224\n\t"
      "v_rcp_f32 v225, v225\n\t"
      "v_add_f32 v226, 1.0, v226\n\t"
      "v_mfma_f32_32x32x16_bf16 v[176:191], v[220:223], v[28:31], v[176:191]\n\t"
      "ds_read_b64_tr_b16 v[212:213], v229 offset:26624\n\t"
      "ds_read_b64_tr_b16 v[214:215], v229 offset:27136\n\t"
      "v_add_f32 v227, 1.0, v227\n\t"
      "v_rcp_f32 v226, v226\n\t"
      "v_rcp_f32 v227, v227\n\t"
      "v_mul_f32 v224, v128, v224\n\t"
      "v_mul_f32 v225, v129, v225\n\t"
      "s_waitcnt lgkmcnt(8)\n\t"
      "v_mfma_f32_32x32x16_bf16 v[144:159], v[192:195], v[32:35], v[144:159]\n\t"
      "s_waitcnt vmcnt(0)\n\t"
      "s_barrier\n\t"
      "ds_read_b64_tr_b16 v[216:217], v229 offset:28672\n\t"
      "ds_read_b64_tr_b16 v[218:219], v229 offset:29184\n\t"
      "v_cvt_pk_bf16_f32 v128, v224, v225\n\t"
      "v_mul_f32 v226, v130, v226\n\t"
      "v_mul_f32 v227, v131, v227\n\t"
      "v_cvt_pk_bf16_f32 v129, v226, v227\n\t"
      "v_add_f32 v132, v132, v236\n\t"
      "v_add_f32 v133, v133, v237\n\t"
      "v_mul_f32 v224, %[nl], v132\n\t"
      "v_mfma_f32_32x32x16_bf16 v[176:191], v[196:199], v[36:39], v[176:191]\n\t"
      "ds_read_b64_tr_b16 v[220:221], v229 offset:30720\n\t"
      "ds_read_b64_tr_b16 v[222:223], v229 offset:31232\n\t"
      "v_mul_f32 v225, %[nl], v133\n\t"
      "v_exp_f32 v224, v224\n\t"
      "v_exp_f32 v225, v225\n\t"
      "v_add_f32 v134, v134, v238\n\t"
      "v_add_f32 v135, v135, v239\n\t"
      "s_waitcnt lgkmcnt(8)\n\t"
      "v_mfma_f32_32x32x16_bf16 v[144:159], v[200:203], v[40:43], v[144:159]\n\t"
      "ds_read_b64_tr_b16 v[192:193], v229 offset:32768\n\t"
      "ds_read_b64_tr_b16 v[194:195], v229 offset:33280\n\t"
      "v_mul_f32 v226, %[nl], v134\n\t"
      "v_mul_f32 v227, %[nl], v135\n\t"
      "v_exp_f32 v226, v226\n\t"
      "v_exp_f32 v227, v227\n\t"
      "v_add_f32 v224, 1.0, v224\n\t"
      "v_mfma_f32_32x32x16_bf16 v[176:191], v[204:207], v[44:47], v[176:191]\n\t"
      "ds_read_b64_tr_b16 v[196:197], v229 offset:34816\n\t"
      "ds_read_b64_tr_b16 v[198:199], v229 offset:35328\n\t"
      "v_add_f32 v225, 1.0, v225\n\t"
      "v_rcp_f32 v224, v224\n\t"
      "v_rcp_f32 v225, v225\n\t"
      "v_add_f32 v226, 1.0, v226\n\t"
      "v_add_f32 v227, 1.0, v227\n\t"
      "s_waitcnt lgkmcnt(8)\n\t"
      "v_mfma_f32_32x32x16_bf16 v[144:159], v[208:211], v[48:51], v[144:159]\n\t"
      "ds_read_b64_tr_b16 v[200:201], v229 offset:36864\n\t"
      "ds_read_b64_tr_b16 v[202:203], v229 offset:37376\n\t"
      "v_rcp_f32 v226, v226\n\t"
      "v_rcp_f32 v227, v227\n\t"
      "v_mul_f32 v224, v132, v224\n\t"
      "v_mul_f32 v225, v133, v225\n\t"
      "v_cvt_pk_bf16_f32 v130, v224, v225\n\t"
      "v_mfma_f32_32x32x16_bf16 v[176:191], v[212:215], v[52:55], v[176:191]\n\t"
      "ds_read_b64_tr_b16 v[204:205], v229 offset:38912\n\t"
      "ds_read_b64_tr_b16 v[206:207], v229 offset:39424\n\t"
      "v_mul_f32 v226, v134, v226\n\t"
      "v_mul_f32 v227, v135, v227\n\t"
      "v_cvt_pk_bf16_f32 v131, v226, v227\n\t"
      "v_add_f32 v136, v136, v240\n\t"
      "v_add_f32 v137, v137, v241\n\t"
      "v_mul_f32 v224, %[nl], v136\n\t"
      "v_mul_f32 v225, %[nl], v137\n\t"
      "s_waitcnt lgkmcnt(8)\n\t"
      "v_mfma_f32_32x32x16_bf16 v[144:159], v[216:219], v[56:59], v[144:159]\n\t"
      "ds_read_b64_tr_b16 v[208:209], v229 offset:40960\n\t"
      "ds_read_b64_tr_b16 v[210:211], v229 offset:41472\n\t"
      "v_exp_f32 v224, v224\n\t"
      "v_exp_f32 v225, v225\n\t"
      "v_add_f32 v138, v138, v242\n\t"
      "v_add_f32 v139, v139, v243\n\t"
      "v_mul_f32 v226, %[nl], v138\n\t"
      "v_mfma_f32_32x32x16_bf16 v[176:191], v[220:223], v[60:63], v[176:191]\n\t"
      "ds_read_b64_tr_b16 v[212:213], v229 offset:43008\n\t"
      "ds_read_b64_tr_b16 v[214:215], v229 offset:43520\n\t"
      "v_mul_f32 v227, %[nl], v139\n\t"
      "v_exp_f32 v226, v226\n\t"
      "v_exp_f32 v227, v227\n\t"
      "v_add_f32 v224, 1.0, v224\n\t"
      "v_add_f32 v225, 1.0, v225\n\t"
      "s_waitcnt lgkmcnt(8)\n\t"
      "v_mfma_f32_32x32x16_bf16 v[144:159], v[192:195], v[64:67], v[144:159]\n\t"
      "ds_read_b64_tr_b16 v[216:217], v229 offset:45056\n\t"
      "ds_read_b64_tr_b16 v[218:219], v229 offset:45568\n\t"
      "v_rcp_f32 v224, v224\n\t"
      "v_rcp_f32 v225, v225\n\t"
      "v_add_f32 v226, 1.0, v226\n\t"
      "v_add_f32 v227, 1.0, v227\n\t"
      "v_mfma_f32_32x32x16_bf16 v[176:191], v[196:199], v[68:71], v[176:191]\n\t"
      "ds_read_b64_tr_b16 v[220:221], v229 offset:47104\n\t"
      "ds_read_b64_tr_b16 v[222:223], v229 offset:47616\n\t"
      "v_rcp_f32 v226, v226\n\t"
      "v_rcp_f32 v227, v227\n\t"
      "v_mul_f32 v224, v136, v224\n\t"
      "v_mul_f32 v225, v137, v225\n\t"
      "v_cvt_pk_bf16_f32 v132, v224, v225\n\t"
      "v_mul_f32 v226, v138, v226\n\t"
      "s_waitcnt lgkmcnt(8)\n\t"
      "v_mfma_f32_32x32x16_bf16 v[144:159], v[200:203], v[72:75], v[144:159]\n\t"
      "ds_read_b64_tr_b16 v[192:193], v229 offset:49152\n\t"
      "ds_read_b64_tr_b16 v[194:195], v229 offset:49664\n\t"
      "v_mul_f32 v227, v139, v227\n\t"
      "v_cvt_pk_bf16_f32 v133, v226, v227\n\t"
      "v_add_f32 v140, v140, v244\n\t"
      "v_add_f32 v141, v141, v245\n\t"
      "v_mul_f32 v224, %[nl], v140\n\t"
      "v_mul_f32 v225, %[nl], v141\n\t"
      "v_mfma_f32_32x32x16_bf16 v[176:191], v[204:207], v[76:79], v[176:191]\n\t"
      "ds_read_b64_tr_b16 v[196:197], v229 offset:51200\n\t"
      "ds_read_b64_tr_b16 v[198:199], v229 offset:51712\n\t"
      "v_exp_f32 v224, v224\n\t"
      "v_exp_f32 v225, v225\n\t"
      "v_add_f32 v142, v142, v246\n\t"
      "v_add_f32 v143, v143, v247\n\t"
      "v_mul_f32 v226, %[nl], v142\n\t"
      "v_mul_f32 v227, %[nl], v143\n\t"
      "s_waitcnt lgkmcnt(8)\n\t"
      "v_mfma_f32_32x32x16_bf16 v[144:159], v[208:211], v[80:83], v[144:159]\n\t"
      "ds_read_b64_tr_b16 v[200:201], v229 offset:53248\n\t"
      "ds_read_b64_tr_b16 v[202:203], v229 offset:53760\n\t"
      "v_exp_f32 v226, v226\n\t"
      "v_exp_f32 v227, v227\n\t"
      "v_add_f32 v224, 1.0, v224\n\t"
      "v_add_f32 v225, 1.0, v225\n\t"
      "v_mfma_f32_32x32x16_bf16 v[176:191], v[212:215], v[84:87], v[176:191]\n\t"
      "ds_read_b64_tr_b16 v[204:205], v229 offset:55296\n\t"
      "ds_read_b64_tr_b16 v[206:207], v229 offset:55808\n\t"
      "v_rcp_f32 v224, v224\n\t"
      "v_rcp_f32 v225, v225\n\t"
      "v_add_f32 v226, 1.0, v226\n\t"
      "v_add_f32 v227, 1.0, v227\n\t"
      "v_rcp_f32 v226, v226\n\t"
      "s_waitcnt lgkmcnt(8)\n\t"
      "v_mfma_f32_32x32x16_bf16 v[144:159], v[216:219], v[88:91], v[144:159]\n\t"
      "ds_read_b64_tr_b16 v[208:209], v229 offset:57344\n\t"
      "ds_read_b64_tr_b16 v[210:211], v229 offset:57856\n\t"
      "v_rcp_f32 v227, v227\n\t"
      "v_mul_f32 v224, v140, v224\n\t"
      "v_mul_f32 v225, v141, v225\n\t"
      "v_cvt_pk_bf16_f32 v134, v224, v225\n\t"
      "v_mul_f32 v226, v142, v226\n\t"
      "v_mul_f32 v227, v143, v227\n\t"
      "v_mfma_f32_32x32x16_bf16 v[176:191], v[220:223], v[92:95], v[176:191]\n\t"
      "ds_read_b64_tr_b16 v[212:213], v229 offset:59392\n\t"
      "ds_read_b64_tr_b16 v[214:215], v229 offset:59904\n\t"
      "v_cvt_pk_bf16_f32 v135, v226, v227\n\t"
      "s_waitcnt lgkmcnt(8)\n\t"
      "v_mfma_f32_32x32x16_bf16 v[144:159], v[192:195], v[96:99], v[144:159]\n\t"
      "ds_read_b64_tr_b16 v[216:217], v229 offset:61440\n\t"
      "ds_read_b64_tr_b16 v[218:219], v229 offset:61952\n\t"
      "v_mfma_f32_32x32x16_bf16 v[176:191], v[196:199], v[100:103], v[176:191]\n\t"
      "ds_read_b64_tr_b16 v[220:221], v229 offset:63488\n\t"
      "ds_read_b64_tr_b16 v[222:223], v229 offset:64000\n\t"
      "s_waitcnt lgkmcnt(8)\n\t"
      "v_mfma_f32_32x32x16_bf16 v[144:159], v[200:203], v[104:107], v[144:159]\n\t"
      "v_mfma_f32_32x32x16_bf16 v[176:191], v[204:207], v[108:111], v[176:191]\n\t"
      "s_waitcnt lgkmcnt(4)\n\t"
      "v_mfma_f32_32x32x16_bf16 v[144:159], v[208:211], v[112:115], v[144:159]\n\t"
      "v_mfma_f32_32x32x16_bf16 v[176:191], v[212:215], v[116:119], v[176:191]\n\t"
      "s_waitcnt lgkmcnt(0)\n\t"
      "v_mfma_f32_32x32x16_bf16 v[144:159], v[216:219], v[120:123], v[144:159]\n\t"
      "v_mfma_f32_32x32x16_bf16 v[176:191], v[220:223], v[124:127], v[176:191]\n\t"
      "s_nop 15\n\t"
      "v_pk_add_f32 v[144:145], v[144:145], v[176:177]\n\t"
      "v_pk_add_f32 v[146:147], v[146:147], v[178:179]\n\t"
      "v_pk_add_f32 v[148:149], v[148:149], v[180:181]\n\t"
      "v_pk_add_f32 v[150:151], v[150:151], v[182:183]\n\t"
      "v_pk_add_f32 v[152:153], v[152:153], v[184:185]\n\t"
      "v_pk_add_f32 v[154:155], v[154:155], v[186:187]\n\t"
      "v_pk_add_f32 v[156:157], v[156:157], v[188:189]\n\t"
      "v_pk_add_f32 v[158:159], v[158:159], v[190:191]\n\t"
          : "=&{v[128:143]}"(acc[0]), "=&{v[144:159]}"(acc[1]), "+{v[160:175]}"(acc[2]), "+{v[176:191]}"(acc[3])
          : XIN, "{v[228:231]}"(wa), [nl] "s"(-LOG2E) : P2CLOB WCLOB);
      dd[0] = acc[2];
      dd[1] = acc[0];
#pragma unroll
      for (int s = 0; s < 8; s++) dd[1][8 + s] = __uint_as_float(silu2(acc[1][2 * s], acc[1][2 * s + 1]));
      const unsigned dh = dh0 + lane_now() * 16;
      da = (u32x4){dh + 4096, dh + 6144, dh + 8192, dh + 10240};
      __builtin_amdgcn_sched_barrier(0);
      asm volatile(".Lcode_b:");
    } else {
      addr();
      asm volatile(
      "s_barrier\n\t"
      "ds_read_b128 v[0:3], v224\n\t"
      "ds_read_b64_tr_b16 v[192:193], v228 offset:16384\n\t"
      "ds_read_b64_tr_b16 v[194:195], v228 offset:16896\n\t"
      "ds_read_b64_tr_b16 v[196:197], v229 offset:16384\n\t"
      "ds_read_b64_tr_b16 v[198:199], v229 offset:16896\n\t"
      "ds_read_b64_tr_b16 v[200:201], v228 offset:24576\n\t"
      "ds_read_b64_tr_b16 v[202:203], v228 offset:25088\n\t"
      "ds_read_b64_tr_b16 v[204:205], v229 offset:24576\n\t"
      "ds_read_b64_tr_b16 v[206:207], v229 offset:25088\n\t"
      "s_waitcnt lgkmcnt(4)\n\t"
      "v_mfma_f32_32x32x16_bf16 v[128:143], v[192:195], v[0:3], 0\n\t"
      "ds_read_b128 v[4:7], v225\n\t"
      "ds_read_b64_tr_b16 v[208:209], v228 offset:18432\n\t"
      "ds_read_b64_tr_b16 v[210:211], v228 offset:18944\n\t"
      "v_mfma_f32_32x32x16_bf16 v[144:159], v[196:199], v[0:3], 0\n\t"
      "ds_read_b64_tr_b16 v[212:213], v229 offset:18432\n\t"
      "ds_read_b64_tr_b16 v[214:215], v229 offset:18944\n\t"
      "ds_read_b64_tr_b16 v[216:217], v228 offset:26624\n\t"
      "s_waitcnt lgkmcnt(6)\n\t"
      "v_mfma_f32_32x32x16_bf16 v[160:175], v[0:3], v[200:203], 0\n\t"
      "ds_read_b64_tr_b16 v[218:219], v228 offset:27136\n\t"
      "ds_read_b64_tr_b16 v[220:221], v229 offset:26624\n\t"
      "ds_read_b64_tr_b16 v[222:223], v229 offset:27136\n\t"
      "v_mfma_f32_32x32x16_bf16 v[176:191], v[0:3], v[204:207], 0\n\t"
      "s_waitcnt lgkmcnt(4)\n\t"
      "v_mfma_f32_32x32x16_bf16 v[128:143], v[208:211], v[4:7], v[128:143]\n\t"
      "ds_read_b128 v[8:11], v226\n\t"
      "ds_read_b64_tr_b16 v[192:193], v228 offset:20480\n\t"
      "ds_read_b64_tr_b16 v[194:195], v228 offset:20992\n\t"
      "v_mfma_f32_32x32x16_bf16 v[144:159], v[212:215], v[4:7], v[144:159]\n\t"
      "ds_read_b64_tr_b16 v[196:197], v229 offset:20480\n\t"
      "ds_read_b64_tr_b16 v[198:199], v229 offset:20992\n\t"
      "ds_read_b64_tr_b16 v[200:201], v228 offset:28672\n\t"
      "s_waitcnt lgkmcnt(6)\n\t"
      "v_mfma_f32_32x32x16_bf16 v[160:175], v[4:7], v[216:219], v[160:175]\n\t"
      "ds_read_b64_tr_b16 v[202:203], v228 offset:29184\n\t"
      "ds_read_b64_tr_b16 v[204:205], v229 offset:28672\n\t"
      "ds_read_b64_tr_b16 v[206:207], v229 offset:29184\n\t"
      "v_mfma_f32_32x32x16_bf16 v[176:191], v[4:7], v[220:223], v[176:191]\n\t"
      "s_waitcnt lgkmcnt(4)\n\t"
      "v_mfma_f32_32x32x16_bf16 v[128:143], v[192:195], v[8:11], v[128:143]\n\t"
      "ds_read_b128 v[12:15], v227\n\t"
      "ds_read_b64_tr_b16 v[208:209], v228 offset:22528\n\t"
      "ds_read_b64_tr_b16 v[210:211], v228 offset:23040\n\t"
      "v_mfma_f32_32x32x16_bf16 v[144:159], v[196:199], v[8:11], v[144:159]\n\t"
      "ds_read_b64_tr_b16 v[212:213], v229 offset:22528\n\t"
      "ds_read_b64_tr_b16 v[214:215], v229 offset:23040\n\t"
      "ds_read_b64_tr_b16 v[216:217], v228 offset:30720\n\t"
      "s_waitcnt lgkmcnt(6)\n\t"
      "v_mfma_f32_32x32x16_bf16 v[160:175], v[8:11], v[200:203], v[160:175]\n\t"
      "ds_read_b64_tr_b16 v[218:219], v228 offset:31232\n\t"
      "ds_read_b64_tr_b16 v[220:221], v229 offset:30720\n\t"
      "ds_read_b64_tr_b16 v[222:223], v229 offset:31232\n\t"
      "v_mfma_f32_32x32x16_bf16 v[176:191], v[8:11], v[204:207], v[176:191]\n\t"
      "v_add_u32 v224, 32768, v224\n\t"
      "v_add_u32 v225, 32768, v225\n\t"
      "v_add_u32 v226, 32768, v226\n\t"
      "v_add_u32 v227, 32768, v227\n\t"
      "s_waitcnt lgkmcnt(4)\n\t"
      "v_mfma_f32_32x32x16_bf16 v[128:143], v[208:211], v[12:15], v[128:143]\n\t"
      "v_add_u32 v228, 32768, v228\n\t"
      "v_add_u32 v229, 32768, v229\n\t"
      "s_waitcnt lgkmcnt(0)\n\t"
      "s_barrier\n\t"
      "ds_read_b128 v[16:19], v224\n\t"
      "ds_read_b64_tr_b16 v[192:193], v228 offset:16384\n\t"
      "ds_read_b64_tr_b16 v[194:195], v228 offset:16896\n\t"
      "v_mfma_f32_32x32x16_bf16 v[144:159], v[212:215], v[12:15], v[144:159]\n\t"
      "ds_read_b64_tr_b16 v[196:197], v229 offset:16384\n\t"
      "ds_read_b64_tr_b16 v[198:199], v229 offset:16896\n\t"
      "ds_read_b64_tr_b16 v[200:201], v228 offset:24576\n\t"
      "v_mfma_f32_32x32x16_bf16 v[160:175], v[12:15], v[216:219], v[160:175]\n\t"
      "ds_read_b64_tr_b16 v[202:203], v228 offset:25088\n\t"
      "ds_read_b64_tr_b16 v[204:205], v229 offset:24576\n\t"
      "ds_read_b64_tr_b16 v[206:207], v229 offset:25088\n\t"
      "v_mfma_f32_32x32x16_bf16 v[176:191], v[12:15], v[220:223], v[176:191]\n\t"
      "s_waitcnt lgkmcnt(4)\n\t"
      "v_mfma_f32_32x32x16_bf16 v[128:143], v[192:195], v[16:19], v[128:143]\n\t"
      "ds_read_b128 v[20:23], v225\n\t"
      "ds_read_b64_tr_b16 v[208:209], v228 offset:18432\n\t"
      "ds_read_b64_tr_b16 v[210:211], v228 offset:18944\n\t"
      "v_mfma_f32_32x32x16_bf16 v[144:159], v[196:199], v[16:19], v[144:159]\n\t"
      "ds_read_b64_tr_b16 v[212:213], v229 offset:18432\n\t"
      "ds_read_b64_tr_b16 v[214:215], v229 offset:18944\n\t"
      "ds_read_b64_tr_b16 v[216:217], v228 offset:26624\n\t"
      "s_waitcnt lgkmcnt(6)\n\t"
      "v_mfma_f32_32x32x16_bf16 v[160:175], v[16:19], v[200:203], v[160:175]\n\t"
      "ds_read_b64_tr_b16 v[218:219], v228 offset:27136\n\t"
      "ds_read_b64_tr_b16 v[220:221], v229 offset:26624\n\t"
      "ds_read_b64_tr_b16 v[222:223], v229 offset:27136\n\t"
      "v_mfma_f32_32x32x16_bf16 v[176:191], v[16:19], v[204:207], v[176:191]\n\t"
      "s_waitcnt lgkmcnt(4)\n\t"
      "v_mfma_f32_32x32x16_bf16 v[128:143], v[208:211], v[20:23], v[128:143]\n\t"
      "ds_read_b128 v[24:27], v226\n\t"
      "ds_read_b64_tr_b16 v[192:193], v228 offset:20480\n\t"
      "ds_read_b64_tr_b16 v[194:195], v228 offset:20992\n\t"
      "v_mfma_f32_32x32x16_bf16 v[144:159], v[212:215], v[20:23], v[144:159]\n\t"
      "ds_read_b64_tr_b16 v[196:197], v229 offset:20480\n\t"
      "ds_read_b64_tr_b16 v[198:199], v229 offset:20992\n\t"
      "ds_read_b64_tr_b16 v[200:201], v228 offset:28672\n\t"
      "s_waitcnt lgkmcnt(6)\n\t"
      "v_mfma_f32_32x32x16_bf16 v[160:175], v[20:23], v[216:219], v[160:175]\n\t"
      "ds_read_b64_tr_b16 v[202:203], v228 offset:29184\n\t"
      "ds_read_b64_tr_b16 v[204:205], v229 offset:28672\n\t"
      "ds_read_b64_tr_b16 v[206:207], v229 offset:29184\n\t"
      "v_mfma_f32_32x32x16_bf16 v[176:191], v[20:23], v[220:223], v[176:191]\n\t"
      "s_waitcnt lgkmcnt(4)\n\t"
      "v_mfma_f32_32x32x16_bf16 v[128:143], v[192:195], v[24:27], v[128:143]\n\t"
      "ds_read_b128 v[28:31], v227\n\t"
      "ds_read_b64_tr_b16 v[208:209], v228 offset:22528\n\t"
      "ds_read_b64_tr_b16 v[210:211], v228 offset:23040\n\t"
      "v_mfma_f32_32x32x16_bf16 v[144:159], v[196:199], v[24:27], v[144:159]\n\t"
      "ds_read_b64_tr_b16 v[212:213], v229 offset:22528\n\t"
      "ds_read_b64_tr_b16 v[214:215], v229 offset:23040\n\t"
      "ds_read_b64_tr_b16 v[216:217], v228 offset:30720\n\t"
      "s_waitcnt lgkmcnt(6)\n\t"
      "v_mfma_f32_32x32x16_bf16 v[160:175], v[24:27], v[200:203], v[160:175]\n\t"
      "ds_read_b64_tr_b16 v[218:219], v228 offset:31232\n\t"
      "ds_read_b64_tr_b16 v[220:221], v229 offset:30720\n\t"
      "ds_read_b64_tr_b16 v[222:223], v229 offset:31232\n\t"
      "v_mfma_f32_32x32x16_bf16 v[176:191], v[24:27], v[204:207], v[176:191]\n\t"
      "v_add_u32 v224, 32768, v224\n\t"
      "v_add_u32 v225, 32768, v225\n\t"
      "v_add_u32 v226, 32768, v226\n\t"
      "v_add_u32 v227, 32768, v227\n\t"
      "s_waitcnt lgkmcnt(4)\n\t"
      "v_mfma_f32_32x32x16_bf16 v[128:143], v[208:211], v[28:31], v[128:143]\n\t"
      "v_add_u32 v228, 32768, v228\n\t"
      "v_add_u32 v229, 32768, v229\n\t"
      "s_waitcnt lgkmcnt(0)\n\t"
      "s_barrier\n\t"
      "ds_read_b128 v[32:35], v224\n\t"
      "ds_read_b64_tr_b16 v[192:193], v228 offset:16384\n\t"
      "ds_read_b64_tr_b16 v[194:195], v228 offset:16896\n\t"
      "v_mfma_f32_32x32x16_bf16 v[144:159], v[212:215], v[28:31], v[144:159]\n\t"
      "ds_read_b64_tr_b16 v[196:197], v229 offset:16384\n\t"
      "ds_read_b64_tr_b16 v[198:199], v229 offset:16896\n\t"
      "ds_read_b64_tr_b16 v[200:201], v228 offset:24576\n\t"
      "v_mfma_f32_32x32x16_bf16 v[160:175], v[28:31], v[216:219], v[160:175]\n\t"
      "ds_read_b64_tr_b16 v[202:203], v228 offset:25088\n\t"
      "ds_read_b64_tr_b16 v[204:205], v229 offset:24576\n\t"
      "ds_read_b64_tr_b16 v[206:207], v229 offset:25088\n\t"
      "v_mfma_f32_32x32x16_bf16 v[176:191], v[28:31], v[220:223], v[176:191]\n\t"
      "s_waitcnt lgkmcnt(4)\n\t"
      "v_mfma_f32_32x32x16_bf16 v[128:143], v[192:195], v[32:35], v[128:143]\n\t"
      "ds_read_b128 v[36:39], v225\n\t"
      "ds_read_b64_tr_b16 v[208:209], v228 offset:18432\n\t"
      "ds_read_b64_tr_b16 v[210:211], v228 offset:18944\n\t"
      "v_mfma_f32_32x32x16_bf16 v[144:159], v[196:199], v[32:35], v[144:159]\n\t"
      "ds_read_b64_tr_b16 v[212:213], v229 offset:18432\n\t"
      "ds_read_b64_tr_b16 v[214:215], v229 offset:18944\n\t"
      "ds_read_b64_tr_b16 v[216:217], v228 offset:26624\n\t"
      "s_waitcnt lgkmcnt(6)\n\t"
      "v_mfma_f32_32x32x16_bf16 v[160:175], v[32:35], v[200:203], v[160:175]\n\t"
      "ds_read_b64_tr_b16 v[218:219], v228 offset:27136\n\t"
      "ds_read_b64_tr_b16 v[220:221], v229 offset:26624\n\t"
      "ds_read_b64_tr_b16 v[222:223], v229 offset:27136\n\t"
      "v_mfma_f32_32x32x16_bf16 v[176:191], v[32:35], v[204:207], v[176:191]\n\t"
      "s_waitcnt lgkmcnt(4)\n\t"
      "v_mfma_f32_32x32x16_bf16 v[128:143], v[208:211], v[36:39], v[128:143]\n\t"
      "ds_read_b128 v[40:43], v226\n\t"
      "ds_read_b64_tr_b16 v[192:193], v228 offset:20480\n\t"
      "ds_read_b64_tr_b16 v[194:195], v228 offset:20992\n\t"
      "v_mfma_f32_32x32x16_bf16 v[144:159], v[212:215], v[36:39], v[144:159]\n\t"
      "ds_read_b64_tr_b16 v[196:197], v229 offset:20480\n\t"
      "ds_read_b64_tr_b16 v[198:199], v229 offset:20992\n\t"
      "ds_read_b64_tr_b16 v[200:201], v228 offset:28672\n\t"
      "s_waitcnt lgkmcnt(6)\n\t"
      "v_mfma_f32_32x32x16_bf16 v[160:175], v[36:39], v[216:219], v[160:175]\n\t"
      "ds_read_b64_tr_b16 v[202:203], v228 offset:29184\n\t"
      "ds_read_b64_tr_b16 v[204:205], v229 offset:28672\n\t"
      "ds_read_b64_tr_b16 v[206:207], v229 offset:29184\n\t"
      "v_mfma_f32_32x32x16_bf16 v[176:191], v[36:39], v[220:223], v[176:191]\n\t"
      "s_waitcnt lgkmcnt(4)\n\t"
      "v_mfma_f32_32x32x16_bf16 v[128:143], v[192:195], v[40:43], v[128:143]\n\t"
      "ds_read_b128 v[44:47], v227\n\t"
      "ds_read_b64_tr_b16 v[208:209], v228 offset:22528\n\t"
      "ds_read_b64_tr_b16 v[210:211], v228 offset:23040\n\t"
      "v_mfma_f32_32x32x16_bf16 v[144:159], v[196:199], v[40:43], v[144:159]\n\t"
      "ds_read_b64_tr_b16 v[212:213], v229 offset:22528\n\t"
      "ds_read_b64_tr_b16 v[214:215], v229 offset:23040\n\t"
      "ds_read_b64_tr_b16 v[216:217], v228 offset:30720\n\t"
      "s_waitcnt lgkmcnt(6)\n\t"
      "v_mfma_f32_32x32x16_bf16 v[160:175], v[40:43], v[200:203], v[160:175]\n\t"
      "ds_read_b64_tr_b16 v[218:219], v228 offset:31232\n\t"
      "ds_read_b64_tr_b16 v[220:221], v229 offset:30720\n\t"
      "ds_read_b64_tr_b16 v[222:223], v229 offset:31232\n\t"
      "v_mfma_f32_32x32x16_bf16 v[176:191], v[40:43], v[204:207], v[176:191]\n\t"
      "v_add_u32 v224, 32768, v224\n\t"
      "v_add_u32 v225, 32768, v225\n\t"
      "v_add_u32 v226, 32768, v226\n\t"
      "v_add_u32 v227, 32768, v227\n\t"
      "s_waitcnt lgkmcnt(4)\n\t"
      "v_mfma_f32_32x32x16_bf16 v[128:143], v[208:211], v[44:47], v[128:143]\n\t"
      "v_add_u32 v228, 32768, v228\n\t"
      "v_add_u32 v229, 32768, v229\n\t"
      "s_waitcnt lgkmcnt(0)\n\t"
      "s_barrier\n\t"
      "ds_read_b128 v[48:51], v224\n\t"
      "ds_read_b64_tr_b16 v[192:193], v228 offset:16384\n\t"
      "ds_read_b64_tr_b16 v[194:195], v228 offset:16896\n\t"
      "v_mfma_f32_32x32x16_bf16 v[144:159], v[212:215], v[44:47], v[144:159]\n\t"
      "ds_read_b64_tr_b16 v[196:197], v229 offset:16384\n\t"
      "ds_read_b64_tr_b16 v[198:199], v229 offset:16896\n\t"
      "ds_read_b64_tr_b16 v[200:201], v228 offset:24576\n\t"
      "v_mfma_f32_32x32x16_bf16 v[160:175], v[44:47], v[216:219], v[160:175]\n\t"
      "ds_read_b64_tr_b16 v[202:203], v228 offset:25088\n\t"
      "ds_read_b64_tr_b16 v[204:205], v229 offset:24576\n\t"
      "ds_read_b64_tr_b16 v[206:207], v229 offset:25088\n\t"
      "v_mfma_f32_32x32x16_bf16 v[176:191], v[44:47], v[220:223], v[176:191]\n\t"
      "s_waitcnt lgkmcnt(4)\n\t"
      "v_mfma_f32_32x32x16_bf16 v[128:143], v[192:195], v[48:51], v[128:143]\n\t"
      "ds_read_b128 v[52:55], v225\n\t"
      "ds_read_b64_tr_b16 v[208:209], v228 offset:18432\n\t"
      "ds_read_b64_tr_b16 v[210:211], v228 offset:18944\n\t"
      "v_mfma_f32_32x32x16_bf16 v[144:159], v[196:199], v[48:51], v[144:159]\n\t"
      "ds_read_b64_tr_b16 v[212:213], v229 offset:18432\n\t"
      "ds_read_b64_tr_b16 v[214:215], v229 offset:18944\n\t"
      "ds_read_b64_tr_b16 v[216:217], v228 offset:26624\n\t"
      "s_waitcnt lgkmcnt(6)\n\t"
      "v_mfma_f32_32x32x16_bf16 v[160:175], v[48:51], v[200:203], v[160:175]\n\t"
      "ds_read_b64_tr_b16 v[218:219], v228 offset:27136\n\t"
      "ds_read_b64_tr_b16 v[220:221], v229 offset:26624\n\t"
      "ds_read_b64_tr_b16 v[222:223], v229 offset:27136\n\t"
      "v_mfma_f32_32x32x16_bf16 v[176:191], v[48:51], v[204:207], v[176:191]\n\t"
      "s_waitcnt lgkmcnt(4)\n\t"
      "v_mfma_f32_32x32x16_bf16 v[128:143], v[208:211], v[52:55], v[128:143]\n\t"
      "ds_read_b128 v[56:59], v226\n\t"
      "ds_read_b64_tr_b16 v[192:193], v228 offset:20480\n\t"
      "ds_read_b64_tr_b16 v[194:195], v228 offset:20992\n\t"
      "v_mfma_f32_32x32x16_bf16 v[144:159], v[212:215], v[52:55], v[144:159]\n\t"
      "ds_read_b64_tr_b16 v[196:197], v229 offset:20480\n\t"
      "ds_read_b64_tr_b16 v[198:199], v229 offset:20992\n\t"
      "ds_read_b64_tr_b16 v[200:201], v228 offset:28672\n\t"
      "s_waitcnt lgkmcnt(6)\n\t"
      "v_mfma_f32_32x32x16_bf16 v[160:175], v[52:55], v[216:219], v[160:175]\n\t"
      "ds_read_b64_tr_b16 v[202:203], v228 offset:29184\n\t"
      "ds_read_b64_tr_b16 v[204:205], v229 offset:28672\n\t"
      "ds_read_b64_tr_b16 v[206:207], v229 offset:29184\n\t"
      "v_mfma_f32_32x32x16_bf16 v[176:191], v[52:55], v[220:223], v[176:191]\n\t"
      "s_waitcnt lgkmcnt(4)\n\t"
      "v_mfma_f32_32x32x16_bf16 v[128:143], v[192:195], v[56:59], v[128:143]\n\t"
      "ds_read_b128 v[60:63], v227\n\t"
      "ds_read_b64_tr_b16 v[208:209], v228 offset:22528\n\t"
      "ds_read_b64_tr_b16 v[210:211], v228 offset:23040\n\t"
      "v_mfma_f32_32x32x16_bf16 v[144:159], v[196:199], v[56:59], v[144:159]\n\t"
      "ds_read_b64_tr_b16 v[212:213], v229 offset:22528\n\t"
      "ds_read_b64_tr_b16 v[214:215], v229 offset:23040\n\t"
      "ds_read_b64_tr_b16 v[216:217], v228 offset:30720\n\t"
      "s_waitcnt lgkmcnt(6)\n\t"
      "v_mfma_f32_32x32x16_bf16 v[160:175], v[56:59], v[200:203], v[160:175]\n\t"
      "ds_read_b64_tr_b16 v[218:219], v228 offset:31232\n\t"
      "ds_read_b64_tr_b16 v[220:221], v229 offset:30720\n\t"
      "ds_read_b64_tr_b16 v[222:223], v229 offset:31232\n\t"
      "v_mfma_f32_32x32x16_bf16 v[176:191], v[56:59], v[204:207], v[176:191]\n\t"
      "v_add_u32 v224, 4294868992, v224\n\t"
      "v_add_u32 v225, 4294868992, v225\n\t"
      "v_add_u32 v226, 4294868992, v226\n\t"
      "v_add_u32 v227, 4294868992, v227\n\t"
      "s_waitcnt lgkmcnt(4)\n\t"
      "v_mfma_f32_32x32x16_bf16 v[128:143], v[208:211], v[60:63], v[128:143]\n\t"
      "v_add_u32 v228, 4294868992, v228\n\t"
      "v_add_u32 v229, 4294868992, v229\n\t"
      "s_waitcnt lgkmcnt(0)\n\t"
      "s_barrier\n\t"
      "ds_read_b128 v[64:67], v224\n\t"
      "ds_read_b64_tr_b16 v[192:193], v228 offset:16384\n\t"
      "ds_read_b64_tr_b16 v[194:195], v228 offset:16896\n\t"
      "v_mfma_f32_32x32x16_bf16 v[144:159], v[212:215], v[60:63], v[144:159]\n\t"
      "ds_read_b64_tr_b16 v[196:197], v229 offset:16384\n\t"
      "ds_read_b64_tr_b16 v[198:199], v229 offset:16896\n\t"
      "ds_read_b64_tr_b16 v[200:201], v228 offset:24576\n\t"
      "v_mfma_f32_32x32x16_bf16 v[160:175], v[60:63], v[216:219], v[160:175]\n\t"
      "ds_read_b64_tr_b16 v[202:203], v228 offset:25088\n\t"
      "ds_read_b64_tr_b16 v[204:205], v229 offset:24576\n\t"
      "ds_read_b64_tr_b16 v[206:207], v229 offset:25088\n\t"
      "v_mfma_f32_32x32x16_bf16 v[176:191], v[60:63], v[220:223], v[176:191]\n\t"
      "s_waitcnt lgkmcnt(4)\n\t"
      "v_mfma_f32_32x32x16_bf16 v[128:143], v[192:195], v[64:67], v[128:143]\n\t"
      "ds_read_b128 v[68:71], v225\n\t"
      "ds_read_b64_tr_b16 v[208:209], v228 offset:18432\n\t"
      "ds_read_b64_tr_b16 v[210:211], v228 offset:18944\n\t"
      "v_mfma_f32_32x32x16_bf16 v[144:159], v[196:199], v[64:67], v[144:159]\n\t"
      "ds_read_b64_tr_b16 v[212:213], v229 offset:18432\n\t"
      "ds_read_b64_tr_b16 v[214:215], v229 offset:18944\n\t"
      "ds_read_b64_tr_b16 v[216:217], v228 offset:26624\n\t"
      "s_waitcnt lgkmcnt(6)\n\t"
      "v_mfma_f32_32x32x16_bf16 v[160:175], v[64:67], v[200:203], v[160:175]\n\t"
      "ds_read_b64_tr_b16 v[218:219], v228 offset:27136\n\t"
      "ds_read_b64_tr_b16 v[220:221], v229 offset:26624\n\t"
      "ds_read_b64_tr_b16 v[222:223], v229 offset:27136\n\t"
      "v_mfma_f32_32x32x16_bf16 v[176:191], v[64:67], v[204:207], v[176:191]\n\t"
      "s_waitcnt lgkmcnt(4)\n\t"
      "v_mfma_f32_32x32x16_bf16 v[128:143], v[208:211], v[68:71], v[128:143]\n\t"
      "ds_read_b128 v[72:75], v226\n\t"
      "ds_read_b64_tr_b16 v[192:193], v228 offset:20480\n\t"
      "ds_read_b64_tr_b16 v[194:195], v228 offset:20992\n\t"
      "v_mfma_f32_32x32x16_bf16 v[144:159], v[212:215], v[68:71], v[144:159]\n\t"
      "ds_read_b64_tr_b16 v[196:197], v229 offset:20480\n\t"
      "ds_read_b64_tr_b16 v[198:199], v229 offset:20992\n\t"
      "ds_read_b64_tr_b16 v[200:201], v228 offset:28672\n\t"
      "s_waitcnt lgkmcnt(6)\n\t"
      "v_mfma_f32_32x32x16_bf16 v[160:175], v[68:71], v[216:219], v[160:175]\n\t"
      "ds_read_b64_tr_b16 v[202:203], v228 offset:29184\n\t"
      "ds_read_b64_tr_b16 v[204:205], v229 offset:28672\n\t"
      "ds_read_b64_tr_b16 v[206:207], v229 offset:29184\n\t"
      "v_mfma_f32_32x32x16_bf16 v[176:191], v[68:71], v[220:223], v[176:191]\n\t"
      "s_waitcnt lgkmcnt(4)\n\t"
      "v_mfma_f32_32x32x16_bf16 v[128:143], v[192:195], v[72:75], v[128:143]\n\t"
      "ds_read_b128 v[76:79], v227\n\t"
      "ds_read_b64_tr_b16 v[208:209], v228 offset:22528\n\t"
      "ds_read_b64_tr_b16 v[210:211], v228 offset:23040\n\t"
      "v_mfma_f32_32x32x16_bf16 v[144:159], v[196:199], v[72:75], v[144:159]\n\t"
      "ds_read_b64_tr_b16 v[212:213], v229 offset:22528\n\t"
      "ds_read_b64_tr_b16 v[214:215], v229 offset:23040\n\t"
      "ds_read_b64_tr_b16 v[216:217], v228 offset:30720\n\t"
      "s_waitcnt lgkmcnt(6)\n\t"
      "v_mfma_f32_32x32x16_bf16 v[160:175], v[72:75], v[200:203], v[160:175]\n\t"
      "ds_read_b64_tr_b16 v[218:219], v228 offset:31232\n\t"
      "ds_read_b64_tr_b16 v[220:221], v229 offset:30720\n\t"
      "ds_read_b64_tr_b16 v[222:223], v229 offset:31232\n\t"
      "v_mfma_f32_32x32x16_bf16 v[176:191], v[72:75], v[204:207], v[176:191]\n\t"
      "v_add_u32 v224, 32768, v224\n\t"
      "v_add_u32 v225, 32768, v225\n\t"
      "v_add_u32 v226, 32768, v226\n\t"
      "v_add_u32 v227, 32768, v227\n\t"
      "s_waitcnt lgkmcnt(4)\n\t"
      "v_mfma_f32_32x32x16_bf16 v[128:143], v[208:211], v[76:79], v[128:143]\n\t"
      "v_add_u32 v228, 32768, v228\n\t"
      "v_add_u32 v229, 32768, v229\n\t"
      "s_waitcnt lgkmcnt(0)\n\t"
      "s_barrier\n\t"
      "ds_read_b128 v[80:83], v224\n\t"
      "ds_read_b64_tr_b16 v[192:193], v228 offset:16384\n\t"
      "ds_read_b64_tr_b16 v[194:195], v228 offset:16896\n\t"
      "v_mfma_f32_32x32x16_bf16 v[144:159], v[212:215], v[76:79], v[144:159]\n\t"
      "ds_read_b64_tr_b16 v[196:197], v229 offset:16384\n\t"
      "ds_read_b64_tr_b16 v[198:199], v229 offset:16896\n\t"
      "ds_read_b64_tr_b16 v[200:201], v228 offset:24576\n\t"
      "v_mfma_f32_32x32x16_bf16 v[160:175], v[76:79], v[216:219], v[160:175]\n\t"
      "ds_read_b64_tr_b16 v[202:203], v228 offset:25088\n\t"
      "ds_read_b64_tr_b16 v[204:205], v229 offset:24576\n\t"
      "ds_read_b64_tr_b16 v[206:207], v229 offset:25088\n\t"
      "v_mfma_f32_32x32x16_bf16 v[176:191], v[76:79], v[220:223], v[176:191]\n\t"
      "s_waitcnt lgkmcnt(4)\n\t"
      "v_mfma_f32_32x32x16_bf16 v[128:143], v[192:195], v[80:83], v[128:143]\n\t"
      "ds_read_b128 v[84:87], v225\n\t"
      "ds_read_b64_tr_b16 v[208:209], v228 offset:18432\n\t"
      "ds_read_b64_tr_b16 v[210:211], v228 offset:18944\n\t"
      "v_mfma_f32_32x32x16_bf16 v[144:159], v[196:199], v[80:83], v[144:159]\n\t"
      "ds_read_b64_tr_b16 v[212:213], v229 offset:18432\n\t"
      "ds_read_b64_tr_b16 v[214:215], v229 offset:18944\n\t"
      "ds_read_b64_tr_b16 v[216:217], v228 offset:26624\n\t"
      "s_waitcnt lgkmcnt(6)\n\t"
      "v_mfma_f32_32x32x16_bf16 v[160:175], v[80:83], v[200:203], v[160:175]\n\t"
      "ds_read_b64_tr_b16 v[218:219], v228 offset:27136\n\t"
      "ds_read_b64_tr_b16 v[220:221], v229 offset:26624\n\t"
      "ds_read_b64_tr_b16 v[222:223], v229 offset:27136\n\t"
      "v_mfma_f32_32x32x16_bf16 v[176:191], v[80:83], v[204:207], v[176:191]\n\t"
      "s_waitcnt lgkmcnt(4)\n\t"
      "v_mfma_f32_32x32x16_bf16 v[128:143], v[208:211], v[84:87], v[128:143]\n\t"
      "ds_read_b128 v[88:91], v226\n\t"
      "ds_read_b64_tr_b16 v[192:193], v228 offset:20480\n\t"
      "ds_read_b64_tr_b16 v[194:195], v228 offset:20992\n\t"
      "v_mfma_f32_32x32x16_bf16 v[144:159], v[212:215], v[84:87], v[144:159]\n\t"
      "ds_read_b64_tr_b16 v[196:197], v229 offset:20480\n\t"
      "ds_read_b64_tr_b16 v[198:199], v229 offset:20992\n\t"
      "ds_read_b64_tr_b16 v[200:201], v228 offset:28672\n\t"
      "s_waitcnt lgkmcnt(6)\n\t"
      "v_mfma_f32_32x32x16_bf16 v[160:175], v[84:87], v[216:219], v[160:175]\n\t"
      "ds_read_b64_tr_b16 v[202:203], v228 offset:29184\n\t"
      "ds_read_b64_tr_b16 v[204:205], v229 offset:28672\n\t"
      "ds_read_b64_tr_b16 v[206:207], v229 offset:29184\n\t"
      "v_mfma_f32_32x32x16_bf16 v[176:191], v[84:87], v[220:223], v[176:191]\n\t"
      "s_waitcnt lgkmcnt(4)\n\t"
      "v_mfma_f32_32x32x16_bf16 v[128:143], v[192:195], v[88:91], v[128:143]\n\t"
      "ds_read_b128 v[92:95], v227\n\t"
      "ds_read_b64_tr_b16 v[208:209], v228 offset:22528\n\t"
      "ds_read_b64_tr_b16 v[210:211], v228 offset:23040\n\t"
      "v_mfma_f32_32x32x16_bf16 v[144:159], v[196:199], v[88:91], v[144:159]\n\t"
      "ds_read_b64_tr_b16 v[212:213], v229 offset:22528\n\t"
      "ds_read_b64_tr_b16 v[214:215], v229 offset:23040\n\t"
      "ds_read_b64_tr_b16 v[216:217], v228 offset:30720\n\t"
      "s_waitcnt lgkmcnt(6)\n\t"
      "v_mfma_f32_32x32x16_bf16 v[160:175], v[88:91], v[200:203], v[160:175]\n\t"
      "ds_read_b64_tr_b16 v[218:219], v228 offset:31232\n\t"
      "ds_read_b64_tr_b16 v[220:221], v229 offset:30720\n\t"
      "ds_read_b64_tr_b16 v[222:223], v229 offset:31232\n\t"
      "v_mfma_f32_32x32x16_bf16 v[176:191], v[88:91], v[204:207], v[176:191]\n\t"
      "v_add_u32 v224, 32768, v224\n\t"
      "v_add_u32 v225, 32768, v225\n\t"
      "v_add_u32 v226, 32768, v226\n\t"
      "v_add_u32 v227, 32768, v227\n\t"
      "s_waitcnt lgkmcnt(4)\n\t"
      "v_mfma_f32_32x32x16_bf16 v[128:143], v[208:211], v[92:95], v[128:143]\n\t"
      "v_add_u32 v228, 32768, v228\n\t"
      "v_add_u32 v229, 32768, v229\n\t"
      "s_waitcnt lgkmcnt(0)\n\t"
      "s_barrier\n\t"
      "ds_read_b128 v[96:99], v224\n\t"
      "ds_read_b64_tr_b16 v[192:193], v228 offset:16384\n\t"
      "ds_read_b64_tr_b16 v[194:195], v228 offset:16896\n\t"
      "v_mfma_f32_32x32x16_bf16 v[144:159], v[212:215], v[92:95], v[144:159]\n\t"
      "ds_read_b64_tr_b16 v[196:197], v229 offset:16384\n\t"
      "ds_read_b64_tr_b16 v[198:199], v229 offset:16896\n\t"
      "ds_read_b64_tr_b16 v[200:201], v228 offset:24576\n\t"
      "v_mfma_f32_32x32x16_bf16 v[160:175], v[92:95], v[216:219], v[160:175]\n\t"
      "ds_read_b64_tr_b16 v[202:203], v228 offset:25088\n\t"
      "ds_read_b64_tr_b16 v[204:205], v229 offset:24576\n\t"
      "ds_read_b64_tr_b16 v[206:207], v229 offset:25088\n\t"
      "v_mfma_f32_32x32x16_bf16 v[176:191], v[92:95], v[220:223], v[176:191]\n\t"
      "s_waitcnt lgkmcnt(4)\n\t"
      "v_mfma_f32_32x32x16_bf16 v[128:143], v[192:195], v[96:99], v[128:143]\n\t"
      "ds_read_b128 v[100:103], v225\n\t"
      "ds_read_b64_tr_b16 v[208:209], v228 offset:18432\n\t"
      "ds_read_b64_tr_b16 v[210:211], v228 offset:18944\n\t"
      "v_mfma_f32_32x32x16_bf16 v[144:159], v[196:199], v[96:99], v[144:159]\n\t"
      "ds_read_b64_tr_b16 v[212:213], v229 offset:18432\n\t"
      "ds_read_b64_tr_b16 v[214:215], v229 offset:18944\n\t"
      "ds_read_b64_tr_b16 v[216:217], v228 offset:26624\n\t"
      "s_waitcnt lgkmcnt(6)\n\t"
      "v_mfma_f32_32x32x16_bf16 v[160:175], v[96:99], v[200:203], v[160:175]\n\t"
      "ds_read_b64_tr_b16 v[218:219], v228 offset:27136\n\t"
      "ds_read_b64_tr_b16 v[220:221], v229 offset:26624\n\t"
      "ds_read_b64_tr_b16 v[222:223], v229 offset:27136\n\t"
      "v_mfma_f32_32x32x16_bf16 v[176:191], v[96:99], v[204:207], v[176:191]\n\t"
      "s_waitcnt lgkmcnt(4)\n\t"
      "v_mfma_f32_32x32x16_bf16 v[128:143], v[208:211], v[100:103], v[128:143]\n\t"
      "ds_read_b128 v[104:107], v226\n\t"
      "ds_read_b64_tr_b16 v[192:193], v228 offset:20480\n\t"
      "ds_read_b64_tr_b16 v[194:195], v228 offset:20992\n\t"
      "v_mfma_f32_32x32x16_bf16 v[144:159], v[212:215], v[100:103], v[144:159]\n\t"
      "ds_read_b64_tr_b16 v[196:197], v229 offset:20480\n\t"
      "ds_read_b64_tr_b16 v[198:199], v229 offset:20992\n\t"
      "ds_read_b64_tr_b16 v[200:201], v228 offset:28672\n\t"
      "s_waitcnt lgkmcnt(6)\n\t"
      "v_mfma_f32_32x32x16_bf16 v[160:175], v[100:103], v[216:219], v[160:175]\n\t"
      "ds_read_b64_tr_b16 v[202:203], v228 offset:29184\n\t"
      "ds_read_b64_tr_b16 v[204:205], v229 offset:28672\n\t"
      "ds_read_b64_tr_b16 v[206:207], v229 offset:29184\n\t"
      "v_mfma_f32_32x32x16_bf16 v[176:191], v[100:103], v[220:223], v[176:191]\n\t"
      "s_waitcnt lgkmcnt(4)\n\t"
      "v_mfma_f32_32x32x16_bf16 v[128:143], v[192:195], v[104:107], v[128:143]\n\t"
      "ds_read_b128 v[108:111], v227\n\t"
      "ds_read_b64_tr_b16 v[208:209], v228 offset:22528\n\t"
      "ds_read_b64_tr_b16 v[210:211], v228 offset:23040\n\t"
      "v_mfma_f32_32x32x16_bf16 v[144:159], v[196:199], v[104:107], v[144:159]\n\t"
      "ds_read_b64_tr_b16 v[212:213], v229 offset:22528\n\t"
      "ds_read_b64_tr_b16 v[214:215], v229 offset:23040\n\t"
      "ds_read_b64_tr_b16 v[216:217], v228 offset:30720\n\t"
      "s_waitcnt lgkmcnt(6)\n\t"
      "v_mfma_f32_32x32x16_bf16 v[160:175], v[104:107], v[200:203], v[160:175]\n\t"
      "ds_read_b64_tr_b16 v[218:219], v228 offset:31232\n\t"
      "ds_read_b64_tr_b16 v[220:221], v229 offset:30720\n\t"
      "ds_read_b64_tr_b16 v[222:223], v229 offset:31232\n\t"
      "v_mfma_f32_32x32x16_bf16 v[176:191], v[104:107], v[204:207], v[176:191]\n\t"
      "v_add_u32 v224, 32768, v224\n\t"
      "v_add_u32 v225, 32768, v225\n\t"
      "v_add_u32 v226, 32768, v226\n\t"
      "v_add_u32 v227, 32768, v227\n\t"
      "s_waitcnt lgkmcnt(4)\n\t"
      "v_mfma_f32_32x32x16_bf16 v[128:143], v[208:211], v[108:111], v[128:143]\n\t"
      "v_add_u32 v228, 32768, v228\n\t"
      "v_add_u32 v229, 32768, v229\n\t"
      "s_waitcnt lgkmcnt(0)\n\t"
      "s_barrier\n\t"
      "ds_read_b128 v[112:115], v224\n\t"
      "ds_read_b64_tr_b16 v[192:193], v228 offset:16384\n\t"
      "ds_read_b64_tr_b16 v[194:195], v228 offset:16896\n\t"
      "v_mfma_f32_32x32x16_bf16 v[144:159], v[212:215], v[108:111], v[144:159]\n\t"
      "ds_read_b64_tr_b16 v[196:197], v229 offset:16384\n\t"
      "ds_read_b64_tr_b16 v[198:199], v229 offset:16896\n\t"
      "ds_read_b64_tr_b16 v[200:201], v228 offset:24576\n\t"
      "v_mfma_f32_32x32x16_bf16 v[160:175], v[108:111], v[216:219], v[160:175]\n\t"
      "ds_read_b64_tr_b16 v[202:203], v228 offset:25088\n\t"
      "ds_read_b64_tr_b16 v[204:205], v229 offset:24576\n\t"
      "ds_read_b64_tr_b16 v[206:207], v229 offset:25088\n\t"
      "v_mfma_f32_32x32x16_bf16 v[176:191], v[108:111], v[220:223], v[176:191]\n\t"
      "s_waitcnt lgkmcnt(4)\n\t"
      "v_mfma_f32_32x32x16_bf16 v[128:143], v[192:195], v[112:115], v[128:143]\n\t"
      "ds_read_b128 v[116:119], v225\n\t"
      "ds_read_b64_tr_b16 v[208:209], v228 offset:18432\n\t"
      "ds_read_b64_tr_b16 v[210:211], v228 offset:18944\n\t"
      "v_mfma_f32_32x32x16_bf16 v[144:159], v[196:199], v[112:115], v[144:159]\n\t"
      "ds_read_b64_tr_b16 v[212:213], v229 offset:18432\n\t"
      "ds_read_b64_tr_b16 v[214:215], v229 offset:18944\n\t"
      "ds_read_b64_tr_b16 v[216:217], v228 offset:26624\n\t"
      "s_waitcnt lgkmcnt(6)\n\t"
      "v_mfma_f32_32x32x16_bf16 v[160:175], v[112:115], v[200:203], v[160:175]\n\t"
      "ds_read_b64_tr_b16 v[218:219], v228 offset:27136\n\t"
      "ds_read_b64_tr_b16 v[220:221], v229 offset:26624\n\t"
      "ds_read_b64_tr_b16 v[222:223], v229 offset:27136\n\t"
      "v_mfma_f32_32x32x16_bf16 v[176:191], v[112:115], v[204:207], v[176:191]\n\t"
      "s_waitcnt lgkmcnt(4)\n\t"
      "v_mfma_f32_32x32x16_bf16 v[128:143], v[208:211], v[116:119], v[128:143]\n\t"
      "ds_read_b128 v[120:123], v226\n\t"
      "ds_read_b64_tr_b16 v[192:193], v228 offset:20480\n\t"
      "ds_read_b64_tr_b16 v[194:195], v228 offset:20992\n\t"
      "v_mfma_f32_32x32x16_bf16 v[144:159], v[212:215], v[116:119], v[144:159]\n\t"
      "ds_read_b64_tr_b16 v[196:197], v229 offset:20480\n\t"
      "ds_read_b64_tr_b16 v[198:199], v229 offset:20992\n\t"
      "ds_read_b64_tr_b16 v[200:201], v228 offset:28672\n\t"
      "s_waitcnt lgkmcnt(6)\n\t"
      "v_mfma_f32_32x32x16_bf16 v[160:175], v[116:119], v[216:219], v[160:175]\n\t"
      "ds_read_b64_tr_b16 v[202:203], v228 offset:29184\n\t"
      "ds_read_b64_tr_b16 v[204:205], v229 offset:28672\n\t"
      "ds_read_b64_tr_b16 v[206:207], v229 offset:29184\n\t"
      "v_mfma_f32_32x32x16_bf16 v[176:191], v[116:119], v[220:223], v[176:191]\n\t"
      "s_waitcnt lgkmcnt(4)\n\t"
      "v_mfma_f32_32x32x16_bf16 v[128:143], v[192:195], v[120:123], v[128:143]\n\t"
      "ds_read_b128 v[124:127], v227\n\t"
      "ds_read_b64_tr_b16 v[208:209], v228 offset:22528\n\t"
      "ds_read_b64_tr_b16 v[210:211], v228 offset:23040\n\t"
      "v_mfma_f32_32x32x16_bf16 v[144:159], v[196:199], v[120:123], v[144:159]\n\t"
      "ds_read_b64_tr_b16 v[212:213], v229 offset:22528\n\t"
      "ds_read_b64_tr_b16 v[214:215], v229 offset:23040\n\t"
      "ds_read_b64_tr_b16 v[216:217], v228 offset:30720\n\t"
      "s_waitcnt lgkmcnt(6)\n\t"
      "v_mfma_f32_32x32x16_bf16 v[160:175], v[120:123], v[200:203], v[160:175]\n\t"
      "ds_read_b64_tr_b16 v[218:219], v228 offset:31232\n\t"
      "ds_read_b64_tr_b16 v[220:221], v229 offset:30720\n\t"
      "ds_read_b64_tr_b16 v[222:223], v229 offset:31232\n\t"
      "v_mfma_f32_32x32x16_bf16 v[176:191], v[120:123], v[204:207], v[176:191]\n\t"
      "s_waitcnt lgkmcnt(4)\n\t"
      "v_mfma_f32_32x32x16_bf16 v[128:143], v[208:211], v[124:127], v[128:143]\n\t"
      "s_waitcnt lgkmcnt(0)\n\t"
      "s_barrier\n\t"
      "v_mfma_f32_32x32x16_bf16 v[144:159], v[212:215], v[124:127], v[144:159]\n\t"
      "v_mfma_f32_32x32x16_bf16 v[160:175], v[124:127], v[216:219], v[160:175]\n\t"
      "v_mfma_f32_32x32x16_bf16 v[176:191], v[124:127], v[220:223], v[176:191]\n\t"
      "s_nop 15\n\t"
          P1OPS);
      LANE();
      const unsigned dk = WS_KF + hb + (((l31 & 19) | ((l31 & 4) << 1) | ((l31 & 8) >> 1)) + 32 * hi) * 16;
      const unsigned dv = WS_VF + hb + (l31 >> 4) * 65536 + hi * 256 + (l31 & 15) * 16;
#pragma unroll
      for (int bk = 0; bk < 2; bk++) {
        WST(pk8(acc[bk], 0, 1.f), dk + bk * 131072);
        WST(pk8(acc[bk], 8, 1.f), dk + bk * 131072 + 65536);
        WST(pk8(acc[2 + bk], 0, 1.f), dv + bk * 131072);
        WST(pk8(acc[2 + bk], 8, 1.f), dv + bk * 131072 + 512);
      }
      wa = (u32x4){wa[2], wa[2] ^ 64, lds0 + 65536 + (8 * hi + ((lane & 15) >> 2)) * 64 + ((lane >> 4) & 1) * 32 + (lane & 3) * 8, 0};
      asm volatile(
      "ds_read_b64_tr_b16 v[192:193], v228 offset:0\n\t"
      "ds_read_b64_tr_b16 v[194:195], v228 offset:512\n\t"
      "ds_read_b64_tr_b16 v[196:197], v228 offset:2048\n\t"
      "ds_read_b64_tr_b16 v[198:199], v228 offset:2560\n\t"
      "ds_read_b64_tr_b16 v[200:201], v228 offset:4096\n\t"
      "ds_read_b64_tr_b16 v[202:203], v228 offset:4608\n\t"
      "ds_read_b64_tr_b16 v[204:205], v228 offset:6144\n\t"
      "ds_read_b64_tr_b16 v[206:207], v228 offset:6656\n\t"
      "ds_read_b64_tr_b16 v[208:209], v228 offset:8192\n\t"
      "ds_read_b64_tr_b16 v[210:211], v228 offset:8704\n\t"
      "ds_read_b64_tr_b16 v[212:213], v228 offset:10240\n\t"
      "ds_read_b64_tr_b16 v[214:215], v228 offset:10752\n\t"
      "s_waitcnt lgkmcnt(8)\n\t"
      "v_mfma_f32_32x32x16_bf16 v[128:143], v[192:195], v[0:3], 0\n\t"
      "ds_read_b64_tr_b16 v[216:217], v228 offset:12288\n\t"
      "ds_read_b64_tr_b16 v[218:219], v228 offset:12800\n\t"
      "v_mfma_f32_32x32x16_bf16 v[160:175], v[196:199], v[4:7], 0\n\t"
      "ds_read_b64_tr_b16 v[220:221], v228 offset:14336\n\t"
      "ds_read_b64_tr_b16 v[222:223], v228 offset:14848\n\t"
      "s_waitcnt lgkmcnt(8)\n\t"
      "v_mfma_f32_32x32x16_bf16 v[128:143], v[200:203], v[8:11], v[128:143]\n\t"
      "ds_read_b64_tr_b16 v[192:193], v228 offset:16384\n\t"
      "ds_read_b64_tr_b16 v[194:195], v228 offset:16896\n\t"
      "v_mfma_f32_32x32x16_bf16 v[160:175], v[204:207], v[12:15], v[160:175]\n\t"
      "ds_read_b64_tr_b16 v[196:197], v228 offset:18432\n\t"
      "ds_read_b64_tr_b16 v[198:199], v228 offset:18944\n\t"
      "s_waitcnt lgkmcnt(8)\n\t"
      "v_mfma_f32_32x32x16_bf16 v[128:143], v[208:211], v[16:19], v[128:143]\n\t"
      "ds_read_b64_tr_b16 v[200:201], v228 offset:20480\n\t"
      "ds_read_b64_tr_b16 v[202:203], v228 offset:20992\n\t"
      "v_mfma_f32_32x32x16_bf16 v[160:175], v[212:215], v[20:23], v[160:175]\n\t"
      "ds_read_b64_tr_b16 v[204:205], v228 offset:22528\n\t"
      "ds_read_b64_tr_b16 v[206:207], v228 offset:23040\n\t"
      "s_waitcnt lgkmcnt(8)\n\t"
      "v_mfma_f32_32x32x16_bf16 v[128:143], v[216:219], v[24:27], v[128:143]\n\t"
      "ds_read_b64_tr_b16 v[208:209], v228 offset:24576\n\t"
      "ds_read_b64_tr_b16 v[210:211], v228 offset:25088\n\t"
      "v_mfma_f32_32x32x16_bf16 v[160:175], v[220:223], v[28:31], v[160:175]\n\t"
      "ds_read_b64_tr_b16 v[212:213], v228 offset:26624\n\t"
      "ds_read_b64_tr_b16 v[214:215], v228 offset:27136\n\t"
      "s_waitcnt lgkmcnt(8)\n\t"
      "v_mfma_f32_32x32x16_bf16 v[128:143], v[192:195], v[32:35], v[128:143]\n\t"
      "ds_read_b64_tr_b16 v[216:217], v228 offset:28672\n\t"
      "ds_read_b64_tr_b16 v[218:219], v228 offset:29184\n\t"
      "v_mfma_f32_32x32x16_bf16 v[160:175], v[196:199], v[36:39], v[160:175]\n\t"
      "ds_read_b64_tr_b16 v[220:221], v228 offset:30720\n\t"
      "ds_read_b64_tr_b16 v[222:223], v228 offset:31232\n\t"
      "s_waitcnt lgkmcnt(8)\n\t"
      "v_mfma_f32_32x32x16_bf16 v[128:143], v[200:203], v[40:43], v[128:143]\n\t"
      "s_barrier\n\t"
      "ds_read_b64_tr_b16 v[192:193], v228 offset:32768\n\t"
      "ds_read_b64_tr_b16 v[194:195], v228 offset:33280\n\t"
      "v_mfma_f32_32x32x16_bf16 v[160:175], v[204:207], v[44:47], v[160:175]\n\t"
      "ds_read_b64_tr_b16 v[196:197], v228 offset:34816\n\t"
      "ds_read_b64_tr_b16 v[198:199], v228 offset:35328\n\t"
      "s_waitcnt lgkmcnt(8)\n\t"
      "v_mfma_f32_32x32x16_bf16 v[128:143], v[208:211], v[48:51], v[128:143]\n\t"
      "ds_read_b64_tr_b16 v[200:201], v228 offset:36864\n\t"
      "ds_read_b64_tr_b16 v[202:203], v228 offset:37376\n\t"
      "v_mfma_f32_32x32x16_bf16 v[160:175], v[212:215], v[52:55], v[160:175]\n\t"
      "ds_read_b64_tr_b16 v[204:205], v228 offset:38912\n\t"
      "ds_read_b64_tr_b16 v[206:207], v228 offset:39424\n\t"
      "s_waitcnt lgkmcnt(8)\n\t"
      "v_mfma_f32_32x32x16_bf16 v[128:143], v[216:219], v[56:59], v[128:143]\n\t"
      "ds_read_b64_tr_b16 v[208:209], v228 offset:40960\n\t"
      "ds_read_b64_tr_b16 v[210:211], v228 offset:41472\n\t"
      "v_mfma_f32_32x32x16_bf16 v[160:175], v[220:223], v[60:63], v[160:175]\n\t"
      "ds_read_b64_tr_b16 v[212:213], v228 offset:43008\n\t"
      "ds_read_b64_tr_b16 v[214:215], v228 offset:43520\n\t"
      "s_waitcnt lgkmcnt(8)\n\t"
      "v_mfma_f32_32x32x16_bf16 v[128:143], v[192:195], v[64:67], v[128:143]\n\t"
      "ds_read_b64_tr_b16 v[216:217], v228 offset:45056\n\t"
      "ds_read_b64_tr_b16 v[218:219], v228 offset:45568\n\t"
      "v_mfma_f32_32x32x16_bf16 v[160:175], v[196:199], v[68:71], v[160:175]\n\t"
      "ds_read_b64_tr_b16 v[220:221], v228 offset:47104\n\t"
      "ds_read_b64_tr_b16 v[222:223], v228 offset:47616\n\t"
      "s_waitcnt lgkmcnt(8)\n\t"
      "v_mfma_f32_32x32x16_bf16 v[128:143], v[200:203], v[72:75], v[128:143]\n\t"
      "ds_read_b64_tr_b16 v[192:193], v228 offset:49152\n\t"
      "ds_read_b64_tr_b16 v[194:195], v228 offset:49664\n\t"
      "v_mfma_f32_32x32x16_bf16 v[160:175], v[204:207], v[76:79], v[160:175]\n\t"
      "ds_read_b64_tr_b16 v[196:197], v228 offset:51200\n\t"
      "ds_read_b64_tr_b16 v[198:199], v228 offset:51712\n\t"
      "s_waitcnt lgkmcnt(8)\n\t"
      "v_mfma_f32_32x32x16_bf16 v[128:143], v[208:211], v[80:83], v[128:143]\n\t"
      "ds_read_b64_tr_b16 v[200:201], v228 offset:53248\n\t"
      "ds_read_b64_tr_b16 v[202:203], v228 offset:53760\n\t"
      "v_mfma_f32_32x32x16_bf16 v[160:175], v[212:215], v[84:87], v[160:175]\n\t"
      "ds_read_b64_tr_b16 v[204:205], v228 offset:55296\n\t"
      "ds_read_b64_tr_b16 v[206:207], v228 offset:55808\n\t"
      "s_waitcnt lgkmcnt(8)\n\t"
      "v_mfma_f32_32x32x16_bf16 v[128:143], v[216:219], v[88:91], v[128:143]\n\t"
      "ds_read_b64_tr_b16 v[208:209], v228 offset:57344\n\t"
      "ds_read_b64_tr_b16 v[210:211], v228 offset:57856\n\t"
      "v_mfma_f32_32x32x16_bf16 v[160:175], v[220:223], v[92:95], v[160:175]\n\t"
      "ds_read_b64_tr_b16 v[212:213], v228 offset:59392\n\t"
      "ds_read_b64_tr_b16 v[214:215], v228 offset:59904\n\t"
      "s_waitcnt lgkmcnt(8)\n\t"
      "v_mfma_f32_32x32x16_bf16 v[128:143], v[192:195], v[96:99], v[128:143]\n\t"
      "ds_read_b64_tr_b16 v[216:217], v228 offset:61440\n\t"
      "ds_read_b64_tr_b16 v[218:219], v228 offset:61952\n\t"
      "v_mfma_f32_32x32x16_bf16 v[160:175], v[196:199], v[100:103], v[160:175]\n\t"
      "ds_read_b64_tr_b16 v[220:221], v228 offset:63488\n\t"
      "ds_read_b64_tr_b16 v[222:223], v228 offset:64000\n\t"
      "s_waitcnt lgkmcnt(8)\n\t"
      "v_mfma_f32_32x32x16_bf16 v[128:143], v[200:203], v[104:107], v[128:143]\n\t"
      "ds_read_b64_tr_b16 v[192:193], v229 offset:0\n\t"
      "ds_read_b64_tr_b16 v[194:195], v229 offset:512\n\t"
      "v_mfma_f32_32x32x16_bf16 v[160:175], v[204:207], v[108:111], v[160:175]\n\t"
      "ds_read_b64_tr_b16 v[196:197], v229 offset:2048\n\t"
      "ds_read_b64_tr_b16 v[198:199], v229 offset:2560\n\t"
      "s_waitcnt lgkmcnt(8)\n\t"
      "v_mfma_f32_32x32x16_bf16 v[128:143], v[208:211], v[112:115], v[128:143]\n\t"
      "ds_read_b64_tr_b16 v[200:201], v229 offset:4096\n\t"
      "ds_read_b64_tr_b16 v[202:203], v229 offset:4608\n\t"
      "v_mfma_f32_32x32x16_bf16 v[160:175], v[212:215], v[116:119], v[160:175]\n\t"
      "ds_read_b64_tr_b16 v[204:205], v229 offset:6144\n\t"
      "ds_read_b64_tr_b16 v[206:207], v229 offset:6656\n\t"
      "s_waitcnt lgkmcnt(8)\n\t"
      "v_mfma_f32_32x32x16_bf16 v[128:143], v[216:219], v[120:123], v[128:143]\n\t"
      "ds_read_b64_tr_b16 v[208:209], v229 offset:8192\n\t"
      "ds_read_b64_tr_b16 v[210:211], v229 offset:8704\n\t"
      "v_mfma_f32_32x32x16_bf16 v[160:175], v[220:223], v[124:127], v[160:175]\n\t"
      "ds_read_b64_tr_b16 v[212:213], v229 offset:10240\n\t"
      "ds_read_b64_tr_b16 v[214:215], v229 offset:10752\n\t"
      "s_waitcnt lgkmcnt(8)\n\t"
      "v_mfma_f32_32x32x16_bf16 v[144:159], v[192:195], v[0:3], 0\n\t"
      "ds_read_b64_tr_b16 v[216:217], v229 offset:12288\n\t"
      "ds_read_b64_tr_b16 v[218:219], v229 offset:12800\n\t"
      "v_mfma_f32_32x32x16_bf16 v[232:247], v[196:199], v[4:7], 0\n\t"
      "ds_read_b64_tr_b16 v[220:221], v229 offset:14336\n\t"
      "ds_read_b64_tr_b16 v[222:223], v229 offset:14848\n\t"
      "s_waitcnt lgkmcnt(8)\n\t"
      "v_mfma_f32_32x32x16_bf16 v[144:159], v[200:203], v[8:11], v[144:159]\n\t"
      "ds_read_b64_tr_b16 v[192:193], v229 offset:16384\n\t"
      "ds_read_b64_tr_b16 v[194:195], v229 offset:16896\n\t"
      "v_mfma_f32_32x32x16_bf16 v[232:247], v[204:207], v[12:15], v[232:247]\n\t"
      "ds_read_b64_tr_b16 v[196:197], v229 offset:18432\n\t"
      "ds_read_b64_tr_b16 v[198:199], v229 offset:18944\n\t"
      "s_waitcnt lgkmcnt(8)\n\t"
      "v_mfma_f32_32x32x16_bf16 v[144:159], v[208:211], v[16:19], v[144:159]\n\t"
      "ds_read_b64_tr_b16 v[200:201], v229 offset:20480\n\t"
      "ds_read_b64_tr_b16 v[202:203], v229 offset:20992\n\t"
      "v_add_f32 v128, v128, v160\n\t"
      "v_add_f32 v129, v129, v161\n\t"
      "v_mul_f32 v224, %[nl], v128\n\t"
      "v_mul_f32 v225, %[nl], v129\n\t"
      "v_exp_f32 v224, v224\n\t"
      "v_mfma_f32_32x32x16_bf16 v[232:247], v[212:215], v[20:23], v[232:247]\n\t"
      "ds_read_b64_tr_b16 v[204:205], v229 offset:22528\n\t"
      "ds_read_b64_tr_b16 v[206:207], v229 offset:23040\n\t"
      "v_exp_f32 v225, v225\n\t"
      "v_add_f32 v130, v130, v162\n\t"
      "v_add_f32 v131, v131, v163\n\t"
      "v_mul_f32 v226, %[nl], v130\n\t"
      "v_mul_f32 v227, %[nl], v131\n\t"
      "s_waitcnt lgkmcnt(8)\n\t"
      "v_mfma_f32_32x32x16_bf16 v[144:159], v[216:219], v[24:27], v[144:159]\n\t"
      "ds_read_b64_tr_b16 v[208:209], v229 offset:24576\n\t"
      "ds_read_b64_tr_b16 v[210:211], v229 offset:25088\n\t"
      "v_exp_f32 v226, v226\n\t"
      "v_exp_f32 v227, v227\n\t"
      "v_add_f32 v224, 1.0, v224\n\t"
      "v_mfma_f32_32x32x16_bf16 v[232:247], v[220:223], v[28:31], v[232:247]\n\t"
      "ds_read_b64_tr_b16 v[212:213], v229 offset:26624\n\t"
      "ds_read_b64_tr_b16 v[214:215], v229 offset:27136\n\t"
      "v_add_f32 v225, 1.0, v225\n\t"
      "v_rcp_f32 v224, v224\n\t"
      "v_rcp_f32 v225, v225\n\t"
      "s_waitcnt lgkmcnt(8)\n\t"
      "v_mfma_f32_32x32x16_bf16 v[144:159], v[192:195], v[32:35], v[144:159]\n\t"
      "s_waitcnt vmcnt(0)\n\t"
      "s_barrier\n\t"
      "ds_read_b64_tr_b16 v[216:217], v229 offset:28672\n\t"
      "ds_read_b64_tr_b16 v[218:219], v229 offset:29184\n\t"
      "v_add_f32 v226, 1.0, v226\n\t"
      "v_add_f32 v227, 1.0, v227\n\t"
      "v_rcp_f32 v226, v226\n\t"
      "v_mfma_f32_32x32x16_bf16 v[232:247], v[196:199], v[36:39], v[232:247]\n\t"
      "ds_read_b64_tr_b16 v[220:221], v229 offset:30720\n\t"
      "ds_read_b64_tr_b16 v[222:223], v229 offset:31232\n\t"
      "v_rcp_f32 v227, v227\n\t"
      "v_mul_f32 v224, v128, v224\n\t"
      "v_mul_f32 v225, v129, v225\n\t"
      "v_cvt_pk_bf16_f32 v128, v224, v225\n\t"
      "v_mul_f32 v226, v130, v226\n\t"
      "s_waitcnt lgkmcnt(8)\n\t"
      "v_mfma_f32_32x32x16_bf16 v[144:159], v[200:203], v[40:43], v[144:159]\n\t"
      "ds_read_b64_tr_b16 v[192:193], v229 offset:32768\n\t"
      "ds_read_b64_tr_b16 v[194:195], v229 offset:33280\n\t"
      "v_mul_f32 v227, v131, v227\n\t"
      "v_cvt_pk_bf16_f32 v129, v226, v227\n\t"
      "v_add_f32 v132, v132, v164\n\t"
      "v_add_f32 v133, v133, v165\n\t"
      "v_mul_f32 v224, %[nl], v132\n\t"
      "v_mfma_f32_32x32x16_bf16 v[232:247], v[204:207], v[44:47], v[232:247]\n\t"
      "ds_read_b64_tr_b16 v[196:197], v229 offset:34816\n\t"
      "ds_read_b64_tr_b16 v[198:199], v229 offset:35328\n\t"
      "v_mul_f32 v225, %[nl], v133\n\t"
      "v_exp_f32 v224, v224\n\t"
      "v_exp_f32 v225, v225\n\t"
      "s_waitcnt lgkmcnt(8)\n\t"
      "v_mfma_f32_32x32x16_bf16 v[144:159], v[208:211], v[48:51], v[144:159]\n\t"
      "ds_read_b64_tr_b16 v[200:201], v229 offset:36864\n\t"
      "ds_read_b64_tr_b16 v[202:203], v229 offset:37376\n\t"
      "v_add_f32 v134, v134, v166\n\t"
      "v_add_f32 v135, v135, v167\n\t"
      "v_mul_f32 v226, %[nl], v134\n\t"
      "v_mul_f32 v227, %[nl], v135\n\t"
      "v_mfma_f32_32x32x16_bf16 v[232:247], v[212:215], v[52:55], v[232:247]\n\t"
      "ds_read_b64_tr_b16 v[204:205], v229 offset:38912\n\t"
      "ds_read_b64_tr_b16 v[206:207], v229 offset:39424\n\t"
      "v_exp_f32 v226, v226\n\t"
      "v_exp_f32 v227, v227\n\t"
      "v_add_f32 v224, 1.0, v224\n\t"
      "v_add_f32 v225, 1.0, v225\n\t"
      "s_waitcnt lgkmcnt(8)\n\t"
      "v_mfma_f32_32x32x16_bf16 v[144:159], v[216:219], v[56:59], v[144:159]\n\t"
      "ds_read_b64_tr_b16 v[208:209], v229 offset:40960\n\t"
      "ds_read_b64_tr_b16 v[210:211], v229 offset:41472\n\t"
      "v_rcp_f32 v224, v224\n\t"
      "v_rcp_f32 v225, v225\n\t"
      "v_add_f32 v226, 1.0, v226\n\t"
      "v_mfma_f32_32x32x16_bf16 v[232:247], v[220:223], v[60:63], v[232:247]\n\t"
      "ds_read_b64_tr_b16 v[212:213], v229 offset:43008\n\t"
      "ds_read_b64_tr_b16 v[214:215], v229 offset:43520\n\t"
      "v_add_f32 v227, 1.0, v227\n\t"
      "v_rcp_f32 v226, v226\n\t"
      "v_rcp_f32 v227, v227\n\t"
      "s_waitcnt lgkmcnt(8)\n\t"
      "v_mfma_f32_32x32x16_bf16 v[144:159], v[192:195], v[64:67], v[144:159]\n\t"
      "ds_read_b64_tr_b16 v[216:217], v229 offset:45056\n\t"
      "ds_read_b64_tr_b16 v[218:219], v229 offset:45568\n\t"
      "v_mul_f32 v224, v132, v224\n\t"
      "v_mul_f32 v225, v133, v225\n\t"
      "v_cvt_pk_bf16_f32 v130, v224, v225\n\t"
      "v_mul_f32 v226, v134, v226\n\t"
      "v_mul_f32 v227, v135, v227\n\t"
      "v_mfma_f32_32x32x16_bf16 v[232:247], v[196:199], v[68:71], v[232:247]\n\t"
      "ds_read_b64_tr_b16 v[220:221], v229 offset:47104\n\t"
      "ds_read_b64_tr_b16 v[222:223], v229 offset:47616\n\t"
      "v_cvt_pk_bf16_f32 v131, v226, v227\n\t"
      "v_add_f32 v136, v136, v168\n\t"
      "v_add_f32 v137, v137, v169\n\t"
      "v_mul_f32 v224, %[nl], v136\n\t"
      "v_mul_f32 v225, %[nl], v137\n\t"
      "s_waitcnt lgkmcnt(8)\n\t"
      "v_mfma_f32_32x32x16_bf16 v[144:159], v[200:203], v[72:75], v[144:159]\n\t"
      "ds_read_b64_tr_b16 v[192:193], v229 offset:49152\n\t"
      "ds_read_b64_tr_b16 v[194:195], v229 offset:49664\n\t"
      "v_exp_f32 v224, v224\n\t"
      "v_exp_f32 v225, v225\n\t"
      "v_add_f32 v138, v138, v170\n\t"
      "v_mfma_f32_32x32x16_bf16 v[232:247], v[204:207], v[76:79], v[232:247]\n\t"
      "ds_read_b64_tr_b16 v[196:197], v229 offset:51200\n\t"
      "ds_read_b64_tr_b16 v[198:199], v229 offset:51712\n\t"
      "v_add_f32 v139, v139, v171\n\t"
      "v_mul_f32 v226, %[nl], v138\n\t"
      "v_mul_f32 v227, %[nl], v139\n\t"
      "v_exp_f32 v226, v226\n\t"
      "s_waitcnt lgkmcnt(8)\n\t"
      "v_mfma_f32_32x32x16_bf16 v[144:159], v[208:211], v[80:83], v[144:159]\n\t"
      "ds_read_b64_tr_b16 v[200:201], v229 offset:53248\n\t"
      "ds_read_b64_tr_b16 v[202:203], v229 offset:53760\n\t"
      "v_exp_f32 v227, v227\n\t"
      "v_add_f32 v224, 1.0, v224\n\t"
      "v_add_f32 v225, 1.0, v225\n\t"
      "v_mfma_f32_32x32x16_bf16 v[232:247], v[212:215], v[84:87], v[232:247]\n\t"
      "ds_read_b64_tr_b16 v[204:205], v229 offset:55296\n\t"
      "ds_read_b64_tr_b16 v[206:207], v229 offset:55808\n\t"
      "v_rcp_f32 v224, v224\n\t"
      "v_rcp_f32 v225, v225\n\t"
      "v_add_f32 v226, 1.0, v226\n\t"
      "v_add_f32 v227, 1.0, v227\n\t"
      "s_waitcnt lgkmcnt(8)\n\t"
      "v_mfma_f32_32x32x16_bf16 v[144:159], v[216:219], v[88:91], v[144:159]\n\t"
      "ds_read_b64_tr_b16 v[208:209], v229 offset:57344\n\t"
      "ds_read_b64_tr_b16 v[210:211], v229 offset:57856\n\t"
      "v_rcp_f32 v226, v226\n\t"
      "v_rcp_f32 v227, v227\n\t"
      "v_mul_f32 v224, v136, v224\n\t"
      "v_mfma_f32_32x32x16_bf16 v[232:247], v[220:223], v[92:95], v[232:247]\n\t"
      "ds_read_b64_tr_b16 v[212:213], v229 offset:59392\n\t"
      "ds_read_b64_tr_b16 v[214:215], v229 offset:59904\n\t"
      "v_mul_f32 v225, v137, v225\n\t"
      "v_cvt_pk_bf16_f32 v132, v224, v225\n\t"
      "v_mul_f32 v226, v138, v226\n\t"
      "v_mul_f32 v227, v139, v227\n\t"
      "v_cvt_pk_bf16_f32 v133, v226, v227\n\t"
      "s_waitcnt lgkmcnt(8)\n\t"
      "v_mfma_f32_32x32x16_bf16 v[144:159], v[192:195], v[96:99], v[144:159]\n\t"
      "ds_read_b64_tr_b16 v[216:217], v229 offset:61440\n\t"
      "ds_read_b64_tr_b16 v[218:219], v229 offset:61952\n\t"
      "v_add_f32 v140, v140, v172\n\t"
      "v_add_f32 v141, v141, v173\n\t"
      "v_mul_f32 v224, %[nl], v140\n\t"
      "v_mul_f32 v225, %[nl], v141\n\t"
      "v_mfma_f32_32x32x16_bf16 v[232:247], v[196:199], v[100:103], v[232:247]\n\t"
      "ds_read_b64_tr_b16 v[220:221], v229 offset:63488\n\t"
      "ds_read_b64_tr_b16 v[222:223], v229 offset:64000\n\t"
      "v_exp_f32 v224, v224\n\t"
      "v_exp_f32 v225, v225\n\t"
      "v_add_f32 v142, v142, v174\n\t"
      "v_add_f32 v143, v143, v175\n\t"
      "s_waitcnt lgkmcnt(8)\n\t"
      "v_mfma_f32_32x32x16_bf16 v[144:159], v[200:203], v[104:107], v[144:159]\n\t"
      "s_barrier\n\t"
      "ds_read_b64_tr_b16 v[192:193], v230 offset:0\n\t"
      "ds_read_b64_tr_b16 v[194:195], v230 offset:256\n\t"
      "v_mul_f32 v226, %[nl], v142\n\t"
      "v_mul_f32 v227, %[nl], v143\n\t"
      "v_exp_f32 v226, v226\n\t"
      "v_mfma_f32_32x32x16_bf16 v[232:247], v[204:207], v[108:111], v[232:247]\n\t"
      "ds_read_b64_tr_b16 v[196:197], v230 offset:1024\n\t"
      "ds_read_b64_tr_b16 v[198:199], v230 offset:1280\n\t"
      "v_exp_f32 v227, v227\n\t"
      "v_add_f32 v224, 1.0, v224\n\t"
      "v_add_f32 v225, 1.0, v225\n\t"
      "v_rcp_f32 v224, v224\n\t"
      "s_waitcnt lgkmcnt(8)\n\t"
      "v_mfma_f32_32x32x16_bf16 v[144:159], v[208:211], v[112:115], v[144:159]\n\t"
      "ds_read_b64_tr_b16 v[200:201], v230 offset:2048\n\t"
      "ds_read_b64_tr_b16 v[202:203], v230 offset:2304\n\t"
      "v_rcp_f32 v225, v225\n\t"
      "v_add_f32 v226, 1.0, v226\n\t"
      "v_add_f32 v227, 1.0, v227\n\t"
      "v_mfma_f32_32x32x16_bf16 v[232:247], v[212:215], v[116:119], v[232:247]\n\t"
      "ds_read_b64_tr_b16 v[204:205], v230 offset:3072\n\t"
      "ds_read_b64_tr_b16 v[206:207], v230 offset:3328\n\t"
      "v_rcp_f32 v226, v226\n\t"
      "v_rcp_f32 v227, v227\n\t"
      "v_mul_f32 v224, v140, v224\n\t"
      "v_mul_f32 v225, v141, v225\n\t"
      "s_waitcnt lgkmcnt(8)\n\t"
      "v_mfma_f32_32x32x16_bf16 v[144:159], v[216:219], v[120:123], v[144:159]\n\t"
      "ds_read_b64_tr_b16 v[208:209], v230 offset:4096\n\t"
      "ds_read_b64_tr_b16 v[210:211], v230 offset:4352\n\t"
      "v_cvt_pk_bf16_f32 v134, v224, v225\n\t"
      "v_mul_f32 v226, v142, v226\n\t"
      "v_mul_f32 v227, v143, v227\n\t"
      "v_cvt_pk_bf16_f32 v135, v226, v227\n\t"
      "v_mfma_f32_32x32x16_bf16 v[232:247], v[220:223], v[124:127], v[232:247]\n\t"
      "ds_read_b64_tr_b16 v[212:213], v230 offset:5120\n\t"
      "ds_read_b64_tr_b16 v[214:215], v230 offset:5376\n\t"
      "s_waitcnt lgkmcnt(8)\n\t"
      "v_mfma_f32_32x32x16_bf16 v[176:191], v[192:195], v[0:3], 0\n\t"
      "ds_read_b64_tr_b16 v[216:217], v230 offset:6144\n\t"
      "ds_read_b64_tr_b16 v[218:219], v230 offset:6400\n\t"
      "v_mfma_f32_32x32x16_bf16 v[160:175], v[196:199], v[4:7], 0\n\t"
      "ds_read_b64_tr_b16 v[220:221], v230 offset:7168\n\t"
      "ds_read_b64_tr_b16 v[222:223], v230 offset:7424\n\t"
      "s_waitcnt lgkmcnt(8)\n\t"
      "v_mfma_f32_32x32x16_bf16 v[176:191], v[200:203], v[8:11], v[176:191]\n\t"
      "ds_read_b64_tr_b16 v[192:193], v230 offset:8192\n\t"
      "ds_read_b64_tr_b16 v[194:195], v230 offset:8448\n\t"
      "v_mfma_f32_32x32x16_bf16 v[160:175], v[204:207], v[12:15], v[160:175]\n\t"
      "ds_read_b64_tr_b16 v[196:197], v230 offset:9216\n\t"
      "ds_read_b64_tr_b16 v[198:199], v230 offset:9472\n\t"
      "s_waitcnt lgkmcnt(8)\n\t"
      "v_mfma_f32_32x32x16_bf16 v[176:191], v[208:211], v[16:19], v[176:191]\n\t"
      "ds_read_b64_tr_b16 v[200:201], v230 offset:10240\n\t"
      "ds_read_b64_tr_b16 v[202:203], v230 offset:10496\n\t"
      "v_add_f32 v144, v144, v232\n\t"
      "v_add_f32 v145, v145, v233\n\t"
      "v_mul_f32 v224, %[nl], v144\n\t"
      "v_mul_f32 v225, %[nl], v145\n\t"
      "v_exp_f32 v224, v224\n\t"
      "v_mfma_f32_32x32x16_bf16 v[160:175], v[212:215], v[20:23], v[160:175]\n\t"
      "ds_read_b64_tr_b16 v[204:205], v230 offset:11264\n\t"
      "ds_read_b64_tr_b16 v[206:207], v230 offset:11520\n\t"
      "v_exp_f32 v225, v225\n\t"
      "v_add_f32 v146, v146, v234\n\t"
      "v_add_f32 v147, v147, v235\n\t"
      "v_mul_f32 v226, %[nl], v146\n\t"
      "v_mul_f32 v227, %[nl], v147\n\t"
      "s_waitcnt lgkmcnt(8)\n\t"
      "v_mfma_f32_32x32x16_bf16 v[176:191], v[216:219], v[24:27], v[176:191]\n\t"
      "ds_read_b64_tr_b16 v[208:209], v230 offset:12288\n\t"
      "ds_read_b64_tr_b16 v[210:211], v230 offset:12544\n\t"
      "v_exp_f32 v226, v226\n\t"
      "v_exp_f32 v227, v227\n\t"
      "v_add_f32 v224, 1.0, v224\n\t"
      "v_mfma_f32_32x32x16_bf16 v[160:175], v[220:223], v[28:31], v[160:175]\n\t"
      "ds_read_b64_tr_b16 v[212:213], v230 offset:13312\n\t"
      "ds_read_b64_tr_b16 v[214:215], v230 offset:13568\n\t"
      "v_add_f32 v225, 1.0, v225\n\t"
      "v_rcp_f32 v224, v224\n\t"
      "v_rcp_f32 v225, v225\n\t"
      "s_waitcnt lgkmcnt(8)\n\t"
      "v_mfma_f32_32x32x16_bf16 v[176:191], v[192:195], v[32:35], v[176:191]\n\t"
      "ds_read_b64_tr_b16 v[216:217], v230 offset:14336\n\t"
      "ds_read_b64_tr_b16 v[218:219], v230 offset:14592\n\t"
      "v_add_f32 v226, 1.0, v226\n\t"
      "v_add_f32 v227, 1.0, v227\n\t"
      "v_rcp_f32 v226, v226\n\t"
      "v_mfma_f32_32x32x16_bf16 v[160:175], v[196:199], v[36:39], v[160:175]\n\t"
      "ds_read_b64_tr_b16 v[220:221], v230 offset:15360\n\t"
      "ds_read_b64_tr_b16 v[222:223], v230 offset:15616\n\t"
      "v_rcp_f32 v227, v227\n\t"
      "v_mul_f32 v224, v144, v224\n\t"
      "v_mul_f32 v225, v145, v225\n\t"
      "v_cvt_pk_bf16_f32 v136, v224, v225\n\t"
      "v_mul_f32 v226, v146, v226\n\t"
      "s_waitcnt lgkmcnt(8)\n\t"
      "v_mfma_f32_32x32x16_bf16 v[176:191], v[200:203], v[40:43], v[176:191]\n\t"
      "ds_read_b64_tr_b16 v[192:193], v230 offset:16384\n\t"
      "ds_read_b64_tr_b16 v[194:195], v230 offset:16640\n\t"
      "v_mul_f32 v227, v147, v227\n\t"
      "v_cvt_pk_bf16_f32 v137, v226, v227\n\t"
      "v_add_f32 v148, v148, v236\n\t"
      "v_add_f32 v149, v149, v237\n\t"
      "v_mul_f32 v224, %[nl], v148\n\t"
      "v_mfma_f32_32x32x16_bf16 v[160:175], v[204:207], v[44:47], v[160:175]\n\t"
      "ds_read_b64_tr_b16 v[196:197], v230 offset:17408\n\t"
      "ds_read_b64_tr_b16 v[198:199], v230 offset:17664\n\t"
      "v_mul_f32 v225, %[nl], v149\n\t"
      "v_exp_f32 v224, v224\n\t"
      "v_exp_f32 v225, v225\n\t"
      "s_waitcnt lgkmcnt(8)\n\t"
      "v_mfma_f32_32x32x16_bf16 v[176:191], v[208:211], v[48:51], v[176:191]\n\t"
      "ds_read_b64_tr_b16 v[200:201], v230 offset:18432\n\t"
      "ds_read_b64_tr_b16 v[202:203], v230 offset:18688\n\t"
      "v_add_f32 v150, v150, v238\n\t"
      "v_add_f32 v151, v151, v239\n\t"
      "v_mul_f32 v226, %[nl], v150\n\t"
      "v_mul_f32 v227, %[nl], v151\n\t"
      "v_mfma_f32_32x32x16_bf16 v[160:175], v[212:215], v[52:55], v[160:175]\n\t"
      "ds_read_b64_tr_b16 v[204:205], v230 offset:19456\n\t"
      "ds_read_b64_tr_b16 v[206:207], v230 offset:19712\n\t"
      "v_exp_f32 v226, v226\n\t"
      "v_exp_f32 v227, v227\n\t"
      "v_add_f32 v224, 1.0, v224\n\t"
      "v_add_f32 v225, 1.0, v225\n\t"
      "s_waitcnt lgkmcnt(8)\n\t"
      "v_mfma_f32_32x32x16_bf16 v[176:191], v[216:219], v[56:59], v[176:191]\n\t"
      "ds_read_b64_tr_b16 v[208:209], v230 offset:20480\n\t"
      "ds_read_b64_tr_b16 v[210:211], v230 offset:20736\n\t"
      "v_rcp_f32 v224, v224\n\t"
      "v_rcp_f32 v225, v225\n\t"
      "v_add_f32 v226, 1.0, v226\n\t"
      "v_mfma_f32_32x32x16_bf16 v[160:175], v[220:223], v[60:63], v[160:175]\n\t"
      "ds_read_b64_tr_b16 v[212:213], v230 offset:21504\n\t"
      "ds_read_b64_tr_b16 v[214:215], v230 offset:21760\n\t"
      "v_add_f32 v227, 1.0, v227\n\t"
      "v_rcp_f32 v226, v226\n\t"
      "v_rcp_f32 v227, v227\n\t"
      "s_waitcnt lgkmcnt(8)\n\t"
      "v_mfma_f32_32x32x16_bf16 v[176:191], v[192:195], v[64:67], v[176:191]\n\t"
      "ds_read_b64_tr_b16 v[216:217], v230 offset:22528\n\t"
      "ds_read_b64_tr_b16 v[218:219], v230 offset:22784\n\t"
      "v_mul_f32 v224, v148, v224\n\t"
      "v_mul_f32 v225, v149, v225\n\t"
      "v_cvt_pk_bf16_f32 v138, v224, v225\n\t"
      "v_mul_f32 v226, v150, v226\n\t"
      "v_mul_f32 v227, v151, v227\n\t"
      "v_mfma_f32_32x32x16_bf16 v[160:175], v[196:199], v[68:71], v[160:175]\n\t"
      "ds_read_b64_tr_b16 v[220:221], v230 offset:23552\n\t"
      "ds_read_b64_tr_b16 v[222:223], v230 offset:23808\n\t"
      "v_cvt_pk_bf16_f32 v139, v226, v227\n\t"
      "v_add_f32 v152, v152, v240\n\t"
      "v_add_f32 v153, v153, v241\n\t"
      "v_mul_f32 v224, %[nl], v152\n\t"
      "v_mul_f32 v225, %[nl], v153\n\t"
      "s_waitcnt lgkmcnt(8)\n\t"
      "v_mfma_f32_32x32x16_bf16 v[176:191], v[200:203], v[72:75], v[176:191]\n\t"
      "ds_read_b64_tr_b16 v[192:193], v230 offset:24576\n\t"
      "ds_read_b64_tr_b16 v[194:195], v230 offset:24832\n\t"
      "v_exp_f32 v224, v224\n\t"
      "v_exp_f32 v225, v225\n\t"
      "v_add_f32 v154, v154, v242\n\t"
      "v_mfma_f32_32x32x16_bf16 v[160:175], v[204:207], v[76:79], v[160:175]\n\t"
      "ds_read_b64_tr_b16 v[196:197], v230 offset:25600\n\t"
      "ds_read_b64_tr_b16 v[198:199], v230 offset:25856\n\t"
      "v_add_f32 v155, v155, v243\n\t"
      "v_mul_f32 v226, %[nl], v154\n\t"
      "v_mul_f32 v227, %[nl], v155\n\t"
      "v_exp_f32 v226, v226\n\t"
      "s_waitcnt lgkmcnt(8)\n\t"
      "v_mfma_f32_32x32x16_bf16 v[176:191], v[208:211], v[80:83], v[176:191]\n\t"
      "ds_read_b64_tr_b16 v[200:201], v230 offset:26624\n\t"
      "ds_read_b64_tr_b16 v[202:203], v230 offset:26880\n\t"
      "v_exp_f32 v227, v227\n\t"
      "v_add_f32 v224, 1.0, v224\n\t"
      "v_add_f32 v225, 1.0, v225\n\t"
      "v_mfma_f32_32x32x16_bf16 v[160:175], v[212:215], v[84:87], v[160:175]\n\t"
      "ds_read_b64_tr_b16 v[204:205], v230 offset:27648\n\t"
      "ds_read_b64_tr_b16 v[206:207], v230 offset:27904\n\t"
      "v_rcp_f32 v224, v224\n\t"
      "v_rcp_f32 v225, v225\n\t"
      "v_add_f32 v226, 1.0, v226\n\t"
      "v_add_f32 v227, 1.0, v227\n\t"
      "s_waitcnt lgkmcnt(8)\n\t"
      "v_mfma_f32_32x32x16_bf16 v[176:191], v[216:219], v[88:91], v[176:191]\n\t"
      "ds_read_b64_tr_b16 v[208:209], v230 offset:28672\n\t"
      "ds_read_b64_tr_b16 v[210:211], v230 offset:28928\n\t"
      "v_rcp_f32 v226, v226\n\t"
      "v_rcp_f32 v227, v227\n\t"
      "v_mul_f32 v224, v152, v224\n\t"
      "v_mfma_f32_32x32x16_bf16 v[160:175], v[220:223], v[92:95], v[160:175]\n\t"
      "ds_read_b64_tr_b16 v[212:213], v230 offset:29696\n\t"
      "ds_read_b64_tr_b16 v[214:215], v230 offset:29952\n\t"
      "v_mul_f32 v225, v153, v225\n\t"
      "v_cvt_pk_bf16_f32 v140, v224, v225\n\t"
      "v_mul_f32 v226, v154, v226\n\t"
      "v_mul_f32 v227, v155, v227\n\t"
      "v_cvt_pk_bf16_f32 v141, v226, v227\n\t"
      "s_waitcnt lgkmcnt(8)\n\t"
      "v_mfma_f32_32x32x16_bf16 v[176:191], v[192:195], v[96:99], v[176:191]\n\t"
      "ds_read_b64_tr_b16 v[216:217], v230 offset:30720\n\t"
      "ds_read_b64_tr_b16 v[218:219], v230 offset:30976\n\t"
      "v_add_f32 v156, v156, v244\n\t"
      "v_add_f32 v157, v157, v245\n\t"
      "v_mul_f32 v224, %[nl], v156\n\t"
      "v_mul_f32 v225, %[nl], v157\n\t"
      "v_mfma_f32_32x32x16_bf16 v[160:175], v[196:199], v[100:103], v[160:175]\n\t"
      "ds_read_b64_tr_b16 v[220:221], v230 offset:31744\n\t"
      "ds_read_b64_tr_b16 v[222:223], v230 offset:32000\n\t"
      "v_exp_f32 v224, v224\n\t"
      "v_exp_f32 v225, v225\n\t"
      "v_add_f32 v158, v158, v246\n\t"
      "v_add_f32 v159, v159, v247\n\t"
      "s_waitcnt lgkmcnt(8)\n\t"
      "v_mfma_f32_32x32x16_bf16 v[176:191], v[200:203], v[104:107], v[176:191]\n\t"
      "v_mul_f32 v226, %[nl], v158\n\t"
      "v_mul_f32 v227, %[nl], v159\n\t"
      "v_exp_f32 v226, v226\n\t"
      "v_mfma_f32_32x32x16_bf16 v[160:175], v[204:207], v[108:111], v[160:175]\n\t"
      "v_exp_f32 v227, v227\n\t"
      "v_add_f32 v224, 1.0, v224\n\t"
      "v_add_f32 v225, 1.0, v225\n\t"
      "v_rcp_f32 v224, v224\n\t"
      "s_waitcnt lgkmcnt(4)\n\t"
      "v_mfma_f32_32x32x16_bf16 v[176:191], v[208:211], v[112:115], v[176:191]\n\t"
      "v_rcp_f32 v225, v225\n\t"
      "v_add_f32 v226, 1.0, v226\n\t"
      "v_add_f32 v227, 1.0, v227\n\t"
      "v_mfma_f32_32x32x16_bf16 v[160:175], v[212:215], v[116:119], v[160:175]\n\t"
      "v_rcp_f32 v226, v226\n\t"
      "v_rcp_f32 v227, v227\n\t"
      "v_mul_f32 v224, v156, v224\n\t"
      "v_mul_f32 v225, v157, v225\n\t"
      "s_waitcnt lgkmcnt(0)\n\t"
      "v_mfma_f32_32x32x16_bf16 v[176:191], v[216:219], v[120:123], v[176:191]\n\t"
      "v_cvt_pk_bf16_f32 v142, v224, v225\n\t"
      "v_mul_f32 v226, v158, v226\n\t"
      "v_mul_f32 v227, v159, v227\n\t"
      "v_cvt_pk_bf16_f32 v143, v226, v227\n\t"
      "v_mfma_f32_32x32x16_bf16 v[160:175], v[220:223], v[124:127], v[160:175]\n\t"
      "s_nop 15\n\t"
      "v_pk_add_f32 v[176:177], v[176:177], v[160:161]\n\t"
      "v_pk_add_f32 v[178:179], v[178:179], v[162:163]\n\t"
      "v_pk_add_f32 v[180:181], v[180:181], v[164:165]\n\t"
      "v_pk_add_f32 v[182:183], v[182:183], v[166:167]\n\t"
      "v_pk_add_f32 v[184:185], v[184:185], v[168:169]\n\t"
      "v_pk_add_f32 v[186:187], v[186:187], v[170:171]\n\t"
      "v_pk_add_f32 v[188:189], v[188:189], v[172:173]\n\t"
      "v_pk_add_f32 v[190:191], v[190:191], v[174:175]\n\t"
          : "=&{v[128:143]}"(acc[0]), "=&{v[144:159]}"(acc[1]), "=&{v[160:175]}"(acc[2]), "=&{v[176:191]}"(acc[3])
          : XIN, "{v[228:231]}"(wa), [nl] "s"(-LOG2E) : P2CLOB WCLOB);
      dd[0] = acc[0];
      dd[1] = acc[1];
#pragma unroll
      for (int s = 0; s < 8; s++) dd[1][s] = __uint_as_float(silu2(acc[3][2 * s], acc[3][2 * s + 1]));
      const unsigned dh = dh0 + lane_now() * 16;
      da[0] = dh + par * 12288;
      da[1] = da[0] + 2048;
      da[2] = dh + (par ? 2048 : 12288);
      __builtin_amdgcn_sched_barrier(0);
      asm volatile(".Lcode_c:");
    }
  }
  LANE();
  RS();
  u32x2 mm = {ma, msg_v};
  u32x4 pp4 = {(unsigned)(unsigned long long)w2, (unsigned)((unsigned long long)w2 >> 32), (unsigned)(unsigned long long)wo,
               (unsigned)((unsigned long long)wo >> 32)}, ad;
  unsigned t1, t2, t3, s_l2, s_l4;
  f32x16 o[2], acc[2] = {dd[0], dd[1]}, hf, h2, h3;
  f32x8 wf;
  float g;
  bf16x8 qf[2];
  asm volatile(
      "s_lshl_b32 %[l2], %[l1], 1\n\t"
      "s_lshl_b32 %[l4], %[l1], 2\n\t"
      "s_and_b32 %[t1], %[l4], 0x3000\n\t"
      "s_add_u32 %[t1], %[t1], 131072\n\t"
      "v_add_u32 v187, %[t1], %[l16]\n\t"
      "s_lshr_b32 %[t2], %[l1], 2\n\t"
      "s_and_b32 %[t2], %[t2], 0x400\n\t"
      "v_add_u32 v186, %[t2], v187\n\t"
      "ds_read_b128 v[176:179], v186\n\t"
      "ds_read_b128 v[172:175], v187 offset:2048\n\t"
      "ds_read_b128 v[180:183], v187 offset:3072\n\t"
      "s_and_b32 %[t1], %[l2], 0x1800\n\t"
      "s_add_u32 %[t1], %[t1], %[t2]\n\t"
      "s_add_u32 %[t1], %[t1], 151552\n\t"
      "v_add_u32 v186, %[t1], %[l16]\n\t"
      "v_xor_b32 v187, 0x400, v186\n\t"
      "v_mov_b64 v[0:1], 0\n\t"
      "v_mov_b64 v[2:3], 0\n\t"
      "v_mov_b64 v[4:5], 0\n\t"
      "v_mov_b64 v[6:7], 0\n\t"
      "v_mov_b64 v[8:9], 0\n\t"
      "v_mov_b64 v[10:11], 0\n\t"
      "v_mov_b64 v[12:13], 0\n\t"
      "v_mov_b64 v[14:15], 0\n\t"
      "v_mov_b64 v[16:17], 0\n\t"
      "v_mov_b64 v[18:19], 0\n\t"
      "v_mov_b64 v[20:21], 0\n\t"
      "v_mov_b64 v[22:23], 0\n\t"
      "v_mov_b64 v[24:25], 0\n\t"
      "v_mov_b64 v[26:27], 0\n\t"
      "v_mov_b64 v[28:29], 0\n\t"
      "v_mov_b64 v[30:31], 0\n\t"
      "v_mov_b64 v[88:89], 0\n\t"
      "v_mov_b64 v[90:91], 0\n\t"
      "v_mov_b64 v[92:93], 0\n\t"
      "v_mov_b64 v[94:95], 0\n\t"
      "s_cmp_eq_u32 %[t2], 0\n\t"
      "s_cbranch_scc1 NF%=\n\t"
      "s_waitcnt vmcnt(0)\n\t"
      "NF%=:\n\t"
      "s_waitcnt lgkmcnt(0)\n\t"
      "s_barrier\n\t"
      "s_setprio 3\n\t"
      "ds_read_b128 v[132:135], v186\n\t"
      "ds_read_b128 v[128:131], v187\n\t"
      "ds_read_b128 v[140:143], %[l16]\n\t"
      "ds_read_b128 v[136:139], v176\n\t"
      "ds_read_b128 v[144:147], v177\n\t"
      "s_add_u32 m0, %[l1], 49152\n\t"
      "s_add_u32 %[t1], %[ko], 8192\n\t"
      "buffer_load_dwordx4 %[l16], %[rs], %[t1] offen sc1 lds\n\t"
      "s_add_u32 m0, %[l1], 57344\n\t"
      "s_add_u32 %[t1], %[ko], 2105344\n\t"
      "buffer_load_dwordx4 %[l16], %[rs], %[t1] offen sc1 lds\n\t"
      "s_waitcnt lgkmcnt(2)\n\t"
      "v_mfma_f32_32x32x16_bf16 v[32:47], v[140:143], v[128:131], 0\n\t"
      "v_mfma_f32_32x32x16_bf16 v[48:63], v[140:143], v[132:135], 0\n\t"
      "ds_read_b128 v[140:143], v177\n\t"
      "s_nop 10\n\t"
      "v_max3_f32 v32, v32, v33, v34\n\t"
      "v_max3_f32 v35, v35, v36, v37\n\t"
      "v_max3_f32 v38, v38, v39, v40\n\t"
      "v_max3_f32 v41, v41, v42, v43\n\t"
      "v_max3_f32 v44, v44, v45, v46\n\t"
      "v_max3_f32 v32, v32, v35, v47\n\t"
      "v_max3_f32 v38, v38, v41, v44\n\t"
      "v_max_f32 v32, v32, v38\n\t"
      "v_mov_b32 v33, v32\n\t"
      "s_nop 1\n\t"
      "v_permlane32_swap_b32 v32, v33\n\t"
      "s_nop 1\n\t"
      "v_max_f32 v32, v32, v33\n\t"
      "v_sub_f32 v32, 0, v32\n\t"
      "v_mov_b32 v33, v32\n\t"
      "v_mov_b64 v[34:35], v[32:33]\n\t"
      "v_mov_b64 v[36:37], v[32:33]\n\t"
      "v_mov_b64 v[38:39], v[32:33]\n\t"
      "v_mov_b64 v[40:41], v[32:33]\n\t"
      "v_mov_b64 v[42:43], v[32:33]\n\t"
      "v_mov_b64 v[44:45], v[32:33]\n\t"
      "v_mov_b64 v[46:47], v[32:33]\n\t"
      "s_waitcnt lgkmcnt(0)\n\t"
      "s_nop 0\n\t"
      "v_mfma_f32_32x32x16_bf16 v[64:79], v[136:139], v[128:131], v[32:47]\n\t"
      "v_max3_f32 v48, v48, v49, v50\n\t"
      "v_max3_f32 v51, v51, v52, v53\n\t"
      "v_max3_f32 v54, v54, v55, v56\n\t"
      "v_max3_f32 v57, v57, v58, v59\n\t"
      "v_max3_f32 v60, v60, v61, v62\n\t"
      "v_max3_f32 v48, v48, v51, v63\n\t"
      "v_max3_f32 v54, v54, v57, v60\n\t"
      "v_max_f32 v48, v48, v54\n\t"
      "v_mov_b32 v49, v48\n\t"
      "s_nop 1\n\t"
      "v_permlane32_swap_b32 v48, v49\n\t"
      "s_nop 1\n\t"
      "v_max_f32 v48, v48, v49\n\t"
      "v_sub_f32 v48, 0, v48\n\t"
      "v_mov_b32 v49, v48\n\t"
      "v_mov_b64 v[50:51], v[48:49]\n\t"
      "v_mov_b64 v[52:53], v[48:49]\n\t"
      "v_mov_b64 v[54:55], v[48:49]\n\t"
      "v_mov_b64 v[56:57], v[48:49]\n\t"
      "v_mov_b64 v[58:59], v[48:49]\n\t"
      "v_mov_b64 v[60:61], v[48:49]\n\t"
      "v_mov_b64 v[62:63], v[48:49]\n\t"
      "v_exp_f32 v64, v64\n\t"
      "v_exp_f32 v65, v65\n\t"
      "v_smfmac_f32_32x32x32_bf16 v[16:31], v[144:147], v[88:95], %[idx]\n\t"
      "ds_read_b128 v[144:147], v177 offset:512\n\t"
      "v_exp_f32 v66, v66\n\t"
      "v_exp_f32 v67, v67\n\t"
      "v_exp_f32 v68, v68\n\t"
      "v_mfma_f32_32x32x16_bf16 v[80:95], v[136:139], v[132:135], v[48:63]\n\t"
      "ds_read_b128 v[136:139], v176 offset:1024\n\t"
      "v_exp_f32 v69, v69\n\t"
      "v_exp_f32 v70, v70\n\t"
      "v_exp_f32 v71, v71\n\t"
      "v_exp_f32 v72, v72\n\t"
      "v_exp_f32 v73, v73\n\t"
      "v_smfmac_f32_32x32x32_bf16 v[0:15], v[140:143], v[64:71], %[idx]\n\t"
      "v_exp_f32 v74, v74\n\t"
      "v_exp_f32 v75, v75\n\t"
      "v_exp_f32 v76, v76\n\t"
      "buffer_store_dwordx4 v[96:99], v188, %[rs], 0 offen offset:0 sc1\n\t"
      "v_exp_f32 v77, v77\n\t"
      "v_exp_f32 v78, v78\n\t"
      "v_exp_f32 v79, v79\n\t"
      "s_setprio 2\n\t"
      "v_exp_f32 v80, v80\n\t"
      "v_exp_f32 v81, v81\n\t"
      "s_waitcnt lgkmcnt(0)\n\t"
      "v_smfmac_f32_32x32x32_bf16 v[0:15], v[144:147], v[72:79], %[idx]\n\t"
      "v_exp_f32 v82, v82\n\t"
      "v_exp_f32 v83, v83\n\t"
      "v_exp_f32 v84, v84\n\t"
      "v_mfma_f32_32x32x16_bf16 v[64:79], v[136:139], v[128:131], v[32:47]\n\t"
      "v_exp_f32 v85, v85\n\t"
      "v_exp_f32 v86, v86\n\t"
      "v_exp_f32 v87, v87\n\t"
      "v_exp_f32 v88, v88\n\t"
      "v_exp_f32 v89, v89\n\t"
      "v_smfmac_f32_32x32x32_bf16 v[16:31], v[140:143], v[80:87], %[idx]\n\t"
      "ds_read_b128 v[140:143], v177 offset:1024\n\t"
      "v_exp_f32 v90, v90\n\t"
      "v_exp_f32 v91, v91\n\t"
      "v_exp_f32 v92, v92\n\t"
      "buffer_store_dwordx4 v[100:103], v188, %[rs], 0 offen offset:1024 sc1\n\t"
      "v_exp_f32 v93, v93\n\t"
      "v_exp_f32 v94, v94\n\t"
      "v_exp_f32 v95, v95\n\t"
      "v_exp_f32 v64, v64\n\t"
      "v_exp_f32 v65, v65\n\t"
      "v_smfmac_f32_32x32x32_bf16 v[16:31], v[144:147], v[88:95], %[idx]\n\t"
      "ds_read_b128 v[144:147], v177 offset:1536\n\t"
      "v_exp_f32 v66, v66\n\t"
      "v_exp_f32 v67, v67\n\t"
      "v_exp_f32 v68, v68\n\t"
      "v_mfma_f32_32x32x16_bf16 v[80:95], v[136:139], v[132:135], v[48:63]\n\t"
      "ds_read_b128 v[136:139], v176 offset:2048\n\t"
      "v_exp_f32 v69, v69\n\t"
      "v_exp_f32 v70, v70\n\t"
      "v_exp_f32 v71, v71\n\t"
      "v_exp_f32 v72, v72\n\t"
      "v_exp_f32 v73, v73\n\t"
      "s_waitcnt lgkmcnt(2)\n\t"
      "v_smfmac_f32_32x32x32_bf16 v[0:15], v[140:143], v[64:71], %[idx]\n\t"
      "v_exp_f32 v74, v74\n\t"
      "v_exp_f32 v75, v75\n\t"
      "v_exp_f32 v76, v76\n\t"
      "buffer_store_dwordx4 v[104:107], v189, %[rs], 0 offen offset:0 sc1\n\t"
      "v_exp_f32 v77, v77\n\t"
      "v_exp_f32 v78, v78\n\t"
      "v_exp_f32 v79, v79\n\t"
      "s_setprio 1\n\t"
      "s_add_u32 s44, s44, %[pw]\n\t"
      "s_addc_u32 s45, s45, 0\n\t"
      "s_add_u32 s46, s46, %[po]\n\t"
      "s_addc_u32 s47, s47, 0\n\t"
      "v_mov_b32 v187, 159744\n\t"
      "ds_read_b32 v182, v187\n\t"
      "v_exp_f32 v80, v80\n\t"
      "v_exp_f32 v81, v81\n\t"
      "s_waitcnt lgkmcnt(1)\n\t"
      "v_smfmac_f32_32x32x32_bf16 v[0:15], v[144:147], v[72:79], %[idx]\n\t"
      "v_exp_f32 v82, v82\n\t"
      "v_exp_f32 v83, v83\n\t"
      "v_exp_f32 v84, v84\n\t"
      "v_mfma_f32_32x32x16_bf16 v[64:79], v[136:139], v[128:131], v[32:47]\n\t"
      "v_exp_f32 v85, v85\n\t"
      "v_exp_f32 v86, v86\n\t"
      "v_exp_f32 v87, v87\n\t"
      "v_exp_f32 v88, v88\n\t"
      "v_exp_f32 v89, v89\n\t"
      "v_smfmac_f32_32x32x32_bf16 v[16:31], v[140:143], v[80:87], %[idx]\n\t"
      "ds_read_b128 v[140:143], v177 offset:2048\n\t"
      "v_exp_f32 v90, v90\n\t"
      "v_exp_f32 v91, v91\n\t"
      "v_exp_f32 v92, v92\n\t"
      "buffer_store_dwordx4 v[108:111], v189, %[rs], 0 offen offset:1024 sc1\n\t"
      "v_exp_f32 v93, v93\n\t"
      "v_exp_f32 v94, v94\n\t"
      "v_exp_f32 v95, v95\n\t"
      "v_exp_f32 v64, v64\n\t"
      "v_exp_f32 v65, v65\n\t"
      "v_smfmac_f32_32x32x32_bf16 v[16:31], v[144:147], v[88:95], %[idx]\n\t"
      "ds_read_b128 v[144:147], v177 offset:2560\n\t"
      "v_exp_f32 v66, v66\n\t"
      "v_exp_f32 v67, v67\n\t"
      "v_exp_f32 v68, v68\n\t"
      "v_mfma_f32_32x32x16_bf16 v[80:95], v[136:139], v[132:135], v[48:63]\n\t"
      "ds_read_b128 v[136:139], v176 offset:3072\n\t"
      "v_exp_f32 v69, v69\n\t"
      "v_exp_f32 v70, v70\n\t"
      "v_exp_f32 v71, v71\n\t"
      "v_exp_f32 v72, v72\n\t"
      "v_exp_f32 v73, v73\n\t"
      "s_waitcnt lgkmcnt(2)\n\t"
      "v_smfmac_f32_32x32x32_bf16 v[0:15], v[140:143], v[64:71], %[idx]\n\t"
      "v_exp_f32 v74, v74\n\t"
      "v_exp_f32 v75, v75\n\t"
      "v_exp_f32 v76, v76\n\t"
      "buffer_store_dwordx4 v[112:115], v190, %[rs], 0 offen offset:0 sc1\n\t"
      "v_exp_f32 v77, v77\n\t"
      "v_exp_f32 v78, v78\n\t"
      "v_exp_f32 v79, v79\n\t"
      "s_setprio 0\n\t"
      "v_exp_f32 v80, v80\n\t"
      "v_exp_f32 v81, v81\n\t"
      "s_waitcnt lgkmcnt(0)\n\t"
      "v_smfmac_f32_32x32x32_bf16 v[0:15], v[144:147], v[72:79], %[idx]\n\t"
      "v_exp_f32 v82, v82\n\t"
      "v_exp_f32 v83, v83\n\t"
      "v_exp_f32 v84, v84\n\t"
      "v_mfma_f32_32x32x16_bf16 v[64:79], v[136:139], v[128:131], v[32:47]\n\t"
      "v_exp_f32 v85, v85\n\t"
      "v_exp_f32 v86, v86\n\t"
      "v_exp_f32 v87, v87\n\t"
      "v_exp_f32 v88, v88\n\t"
      "v_exp_f32 v89, v89\n\t"
      "v_smfmac_f32_32x32x32_bf16 v[16:31], v[140:143], v[80:87], %[idx]\n\t"
      "ds_read_b128 v[140:143], v177 offset:3072\n\t"
      "v_exp_f32 v90, v90\n\t"
      "v_exp_f32 v91, v91\n\t"
      "v_exp_f32 v92, v92\n\t"
      "buffer_store_dwordx4 v[116:119], v190, %[rs], 0 offen offset:1024 sc1\n\t"
      "v_exp_f32 v93, v93\n\t"
      "v_exp_f32 v94, v94\n\t"
      "v_exp_f32 v95, v95\n\t"
      "s_cmp_eq_u32 %[l1], 0x1c00\n\t"
      "s_cselect_b32 %[t1], 63, 0\n\t"
      "v_or_b32 v181, %[t1], v181\n\t"
      "v_or_b32 v182, 0x20000, v182\n\t"
      "v_and_b32 v182, v182, v181\n\t"
      "v_cmp_eq_u32 vcc, 0, v181\n\t"
      "v_bfrev_b32 v187, 1\n\t"
      "s_nop 0\n\t"
      "v_cndmask_b32 v183, v183, v187, vcc\n\t"
      "v_exp_f32 v64, v64\n\t"
      "v_exp_f32 v65, v65\n\t"
      "v_smfmac_f32_32x32x32_bf16 v[16:31], v[144:147], v[88:95], %[idx]\n\t"
      "ds_read_b128 v[144:147], v177 offset:3584\n\t"
      "v_exp_f32 v66, v66\n\t"
      "v_exp_f32 v67, v67\n\t"
      "v_exp_f32 v68, v68\n\t"
      "v_mfma_f32_32x32x16_bf16 v[80:95], v[136:139], v[132:135], v[48:63]\n\t"
      "s_waitcnt vmcnt(6)\n\t"
      "s_barrier\n\t"
      "s_setprio 3\n\t"
      "v_add_u32 v176, 49152, v176\n\t"
      "ds_read_b128 v[136:139], v176 offset:0\n\t"
      "v_exp_f32 v69, v69\n\t"
      "v_exp_f32 v70, v70\n\t"
      "v_exp_f32 v71, v71\n\t"
      "v_exp_f32 v72, v72\n\t"
      "v_exp_f32 v73, v73\n\t"
      "s_waitcnt lgkmcnt(2)\n\t"
      "v_smfmac_f32_32x32x32_bf16 v[0:15], v[140:143], v[64:71], %[idx]\n\t"
      "v_exp_f32 v74, v74\n\t"
      "v_exp_f32 v75, v75\n\t"
      "v_exp_f32 v76, v76\n\t"
      "buffer_store_dwordx4 v[120:123], v191, %[rs], 0 offen offset:0 sc1\n\t"
      "v_exp_f32 v77, v77\n\t"
      "v_exp_f32 v78, v78\n\t"
      "v_exp_f32 v79, v79\n\t"
      "s_add_u32 m0, %[l1], 98304\n\t"
      "s_add_u32 %[t1], %[ko], 16384\n\t"
      "buffer_load_dwordx4 %[l16], %[rs], %[t1] offen sc1 lds\n\t"
      "v_exp_f32 v80, v80\n\t"
      "v_exp_f32 v81, v81\n\t"
      "s_waitcnt lgkmcnt(0)\n\t"
      "v_smfmac_f32_32x32x32_bf16 v[0:15], v[144:147], v[72:79], %[idx]\n\t"
      "v_exp_f32 v82, v82\n\t"
      "v_exp_f32 v83, v83\n\t"
      "v_exp_f32 v84, v84\n\t"
      "v_mfma_f32_32x32x16_bf16 v[64:79], v[136:139], v[128:131], v[32:47]\n\t"
      "v_exp_f32 v85, v85\n\t"
      "v_exp_f32 v86, v86\n\t"
      "v_exp_f32 v87, v87\n\t"
      "v_exp_f32 v88, v88\n\t"
      "v_exp_f32 v89, v89\n\t"
      "v_smfmac_f32_32x32x32_bf16 v[16:31], v[140:143], v[80:87], %[idx]\n\t"
      "s_mov_b32 %[t1], 49152\n\t"
      "v_mad_i32_i24 v177, %[t1], v180, v177\n\t"
      "ds_read_b128 v[140:143], v177 offset:0\n\t"
      "v_exp_f32 v90, v90\n\t"
      "v_exp_f32 v91, v91\n\t"
      "v_exp_f32 v92, v92\n\t"
      "buffer_store_dwordx4 v[124:127], v191, %[rs], 0 offen offset:1024 sc1\n\t"
      "v_exp_f32 v93, v93\n\t"
      "v_exp_f32 v94, v94\n\t"
      "v_exp_f32 v95, v95\n\t"
      "s_add_u32 m0, %[l1], 106496\n\t"
      "s_add_u32 %[t1], %[ko], 2113536\n\t"
      "buffer_load_dwordx4 %[l16], %[rs], %[t1] offen sc1 lds\n\t"
      "v_exp_f32 v64, v64\n\t"
      "v_exp_f32 v65, v65\n\t"
      "v_smfmac_f32_32x32x32_bf16 v[16:31], v[144:147], v[88:95], %[idx]\n\t"
      "ds_read_b128 v[144:147], v177 offset:512\n\t"
      "v_exp_f32 v66, v66\n\t"
      "v_exp_f32 v67, v67\n\t"
      "v_exp_f32 v68, v68\n\t"
      "v_mfma_f32_32x32x16_bf16 v[80:95], v[136:139], v[132:135], v[48:63]\n\t"
      "ds_read_b128 v[136:139], v176 offset:1024\n\t"
      "v_exp_f32 v69, v69\n\t"
      "v_exp_f32 v70, v70\n\t"
      "v_exp_f32 v71, v71\n\t"
      "v_exp_f32 v72, v72\n\t"
      "v_exp_f32 v73, v73\n\t"
      "s_waitcnt lgkmcnt(2)\n\t"
      "v_smfmac_f32_32x32x32_bf16 v[0:15], v[140:143], v[64:71], %[idx]\n\t"
      "v_exp_f32 v74, v74\n\t"
      "v_exp_f32 v75, v75\n\t"
      "v_exp_f32 v76, v76\n\t"
      "v_exp_f32 v77, v77\n\t"
      "v_exp_f32 v78, v78\n\t"
      "v_exp_f32 v79, v79\n\t"
      "s_setprio 2\n\t"
      "s_add_u32 %[t1], %[gr], 0\n\t"
      "s_cmp_ge_u32 %[t1], 7\n\t"
      "s_cselect_b32 vcc_lo, 7, 0\n\t"
      "s_sub_u32 %[t1], %[t1], vcc_lo\n\t"
      "s_lshl_b32 %[t1], %[t1], 16\n\t"
      "s_add_u32 s48, s44, %[t1]\n\t"
      "s_addc_u32 s49, s45, 0\n\t"
      "s_add_u32 m0, %[l4], 16384\n\t"
      "s_nop 0\n\t"
      "global_load_lds_dwordx4 v172, s[48:49]\n\t"
      "v_exp_f32 v80, v80\n\t"
      "v_exp_f32 v81, v81\n\t"
      "s_waitcnt lgkmcnt(0)\n\t"
      "v_smfmac_f32_32x32x32_bf16 v[0:15], v[144:147], v[72:79], %[idx]\n\t"
      "v_exp_f32 v82, v82\n\t"
      "v_exp_f32 v83, v83\n\t"
      "v_exp_f32 v84, v84\n\t"
      "v_mfma_f32_32x32x16_bf16 v[64:79], v[136:139], v[128:131], v[32:47]\n\t"
      "v_exp_f32 v85, v85\n\t"
      "v_exp_f32 v86, v86\n\t"
      "v_exp_f32 v87, v87\n\t"
      "v_exp_f32 v88, v88\n\t"
      "v_exp_f32 v89, v89\n\t"
      "v_smfmac_f32_32x32x32_bf16 v[16:31], v[140:143], v[80:87], %[idx]\n\t"
      "ds_read_b128 v[140:143], v177 offset:1024\n\t"
      "v_exp_f32 v90, v90\n\t"
      "v_exp_f32 v91, v91\n\t"
      "v_exp_f32 v92, v92\n\t"
      "v_exp_f32 v93, v93\n\t"
      "v_exp_f32 v94, v94\n\t"
      "v_exp_f32 v95, v95\n\t"
      "v_exp_f32 v64, v64\n\t"
      "v_exp_f32 v65, v65\n\t"
      "v_smfmac_f32_32x32x32_bf16 v[16:31], v[144:147], v[88:95], %[idx]\n\t"
      "ds_read_b128 v[144:147], v177 offset:1536\n\t"
      "v_exp_f32 v66, v66\n\t"
      "v_exp_f32 v67, v67\n\t"
      "v_exp_f32 v68, v68\n\t"
      "v_mfma_f32_32x32x16_bf16 v[80:95], v[136:139], v[132:135], v[48:63]\n\t"
      "ds_read_b128 v[136:139], v176 offset:2048\n\t"
      "v_exp_f32 v69, v69\n\t"
      "v_exp_f32 v70, v70\n\t"
      "v_exp_f32 v71, v71\n\t"
      "v_exp_f32 v72, v72\n\t"
      "v_exp_f32 v73, v73\n\t"
      "s_waitcnt lgkmcnt(2)\n\t"
      "v_smfmac_f32_32x32x32_bf16 v[0:15], v[140:143], v[64:71], %[idx]\n\t"
      "v_exp_f32 v74, v74\n\t"
      "v_exp_f32 v75, v75\n\t"
      "v_exp_f32 v76, v76\n\t"
      "v_exp_f32 v77, v77\n\t"
      "v_exp_f32 v78, v78\n\t"
      "v_exp_f32 v79, v79\n\t"
      "s_setprio 1\n\t"
      "s_add_u32 m0, %[l4], 17408\n\t"
      "s_nop 0\n\t"
      "global_load_lds_dwordx4 v173, s[48:49]\n\t"
      "v_or_b32 v185, 0x20000, v185\n\t"
      "s_waitcnt vmcnt(3)\n\t"
      "s_barrier\n\t"
      "buffer_store_dword v185, v184, %[rs], 0 offen sc1\n\t"
      "v_exp_f32 v80, v80\n\t"
      "v_exp_f32 v81, v81\n\t"
      "s_waitcnt lgkmcnt(0)\n\t"
      "v_smfmac_f32_32x32x32_bf16 v[0:15], v[144:147], v[72:79], %[idx]\n\t"
      "v_exp_f32 v82, v82\n\t"
      "v_exp_f32 v83, v83\n\t"
      "v_exp_f32 v84, v84\n\t"
      "v_mfma_f32_32x32x16_bf16 v[64:79], v[136:139], v[128:131], v[32:47]\n\t"
      "v_exp_f32 v85, v85\n\t"
      "v_exp_f32 v86, v86\n\t"
      "v_exp_f32 v87, v87\n\t"
      "v_exp_f32 v88, v88\n\t"
      "v_exp_f32 v89, v89\n\t"
      "v_smfmac_f32_32x32x32_bf16 v[16:31], v[140:143], v[80:87], %[idx]\n\t"
      "ds_read_b128 v[140:143], v177 offset:2048\n\t"
      "v_exp_f32 v90, v90\n\t"
      "v_exp_f32 v91, v91\n\t"
      "v_exp_f32 v92, v92\n\t"
      "v_exp_f32 v93, v93\n\t"
      "v_exp_f32 v94, v94\n\t"
      "v_exp_f32 v95, v95\n\t"
      "s_add_u32 m0, %[l4], 18432\n\t"
      "s_nop 0\n\t"
      "global_load_lds_dwordx4 v174, s[48:49]\n\t"
      "v_exp_f32 v64, v64\n\t"
      "v_exp_f32 v65, v65\n\t"
      "v_smfmac_f32_32x32x32_bf16 v[16:31], v[144:147], v[88:95], %[idx]\n\t"
      "ds_read_b128 v[144:147], v177 offset:2560\n\t"
      "v_exp_f32 v66, v66\n\t"
      "v_exp_f32 v67, v67\n\t"
      "v_exp_f32 v68, v68\n\t"
      "v_mfma_f32_32x32x16_bf16 v[80:95], v[136:139], v[132:135], v[48:63]\n\t"
      "ds_read_b128 v[136:139], v176 offset:3072\n\t"
      "v_exp_f32 v69, v69\n\t"
      "v_exp_f32 v70, v70\n\t"
      "v_exp_f32 v71, v71\n\t"
      "v_exp_f32 v72, v72\n\t"
      "v_exp_f32 v73, v73\n\t"
      "s_waitcnt lgkmcnt(2)\n\t"
      "v_smfmac_f32_32x32x32_bf16 v[0:15], v[140:143], v[64:71], %[idx]\n\t"
      "v_exp_f32 v74, v74\n\t"
      "v_exp_f32 v75, v75\n\t"
      "v_exp_f32 v76, v76\n\t"
      "v_exp_f32 v77, v77\n\t"
      "v_exp_f32 v78, v78\n\t"
      "v_exp_f32 v79, v79\n\t"
      "s_setprio 0\n\t"
      "s_add_u32 m0, %[l4], 19456\n\t"
      "s_nop 0\n\t"
      "global_load_lds_dwordx4 v175, s[48:49]\n\t"
      "v_exp_f32 v80, v80\n\t"
      "v_exp_f32 v81, v81\n\t"
      "s_waitcnt lgkmcnt(0)\n\t"
      "v_smfmac_f32_32x32x32_bf16 v[0:15], v[144:147], v[72:79], %[idx]\n\t"
      "v_exp_f32 v82, v82\n\t"
      "v_exp_f32 v83, v83\n\t"
      "v_exp_f32 v84, v84\n\t"
      "v_mfma_f32_32x32x16_bf16 v[64:79], v[136:139], v[128:131], v[32:47]\n\t"
      "v_exp_f32 v85, v85\n\t"
      "v_exp_f32 v86, v86\n\t"
      "v_exp_f32 v87, v87\n\t"
      "v_exp_f32 v88, v88\n\t"
      "v_exp_f32 v89, v89\n\t"
      "v_smfmac_f32_32x32x32_bf16 v[16:31], v[140:143], v[80:87], %[idx]\n\t"
      "ds_read_b128 v[140:143], v177 offset:3072\n\t"
      "v_exp_f32 v90, v90\n\t"
      "v_exp_f32 v91, v91\n\t"
      "v_exp_f32 v92, v92\n\t"
      "v_exp_f32 v93, v93\n\t"
      "v_exp_f32 v94, v94\n\t"
      "v_exp_f32 v95, v95\n\t"
      "v_exp_f32 v64, v64\n\t"
      "v_exp_f32 v65, v65\n\t"
      "v_smfmac_f32_32x32x32_bf16 v[16:31], v[144:147], v[88:95], %[idx]\n\t"
      "ds_read_b128 v[144:147], v177 offset:3584\n\t"
      "v_exp_f32 v66, v66\n\t"
      "v_exp_f32 v67, v67\n\t"
      "v_exp_f32 v68, v68\n\t"
      "v_mfma_f32_32x32x16_bf16 v[80:95], v[136:139], v[132:135], v[48:63]\n\t"
      "s_waitcnt vmcnt(5)\n\t"
      "s_barrier\n\t"
      "s_setprio 3\n\t"
      "v_add_u32 v176, 49152, v176\n\t"
      "ds_read_b128 v[136:139], v176 offset:0\n\t"
      "v_exp_f32 v69, v69\n\t"
      "v_exp_f32 v70, v70\n\t"
      "v_exp_f32 v71, v71\n\t"
      "v_exp_f32 v72, v72\n\t"
      "v_exp_f32 v73, v73\n\t"
      "s_waitcnt lgkmcnt(2)\n\t"
      "v_smfmac_f32_32x32x32_bf16 v[0:15], v[140:143], v[64:71], %[idx]\n\t"
      "v_exp_f32 v74, v74\n\t"
      "v_exp_f32 v75, v75\n\t"
      "v_exp_f32 v76, v76\n\t"
      "v_exp_f32 v77, v77\n\t"
      "v_exp_f32 v78, v78\n\t"
      "v_exp_f32 v79, v79\n\t"
      "s_add_u32 m0, %[l1], 0\n\t"
      "s_add_u32 %[t1], %[ko], 24576\n\t"
      "buffer_load_dwordx4 %[l16], %[rs], %[t1] offen sc1 lds\n\t"
      "v_exp_f32 v80, v80\n\t"
      "v_exp_f32 v81, v81\n\t"
      "s_waitcnt lgkmcnt(0)\n\t"
      "v_smfmac_f32_32x32x32_bf16 v[0:15], v[144:147], v[72:79], %[idx]\n\t"
      "v_exp_f32 v82, v82\n\t"
      "v_exp_f32 v83, v83\n\t"
      "v_exp_f32 v84, v84\n\t"
      "v_mfma_f32_32x32x16_bf16 v[64:79], v[136:139], v[128:131], v[32:47]\n\t"
      "v_exp_f32 v85, v85\n\t"
      "v_exp_f32 v86, v86\n\t"
      "v_exp_f32 v87, v87\n\t"
      "v_exp_f32 v88, v88\n\t"
      "v_exp_f32 v89, v89\n\t"
      "v_smfmac_f32_32x32x32_bf16 v[16:31], v[140:143], v[80:87], %[idx]\n\t"
      "s_mov_b32 %[t1], 49152\n\t"
      "v_mad_i32_i24 v177, %[t1], v180, v177\n\t"
      "ds_read_b128 v[140:143], v177 offset:0\n\t"
      "v_exp_f32 v90, v90\n\t"
      "v_exp_f32 v91, v91\n\t"
      "v_exp_f32 v92, v92\n\t"
      "v_exp_f32 v93, v93\n\t"
      "v_exp_f32 v94, v94\n\t"
      "v_exp_f32 v95, v95\n\t"
      "s_add_u32 m0, %[l1], 8192\n\t"
      "s_add_u32 %[t1], %[ko], 2121728\n\t"
      "buffer_load_dwordx4 %[l16], %[rs], %[t1] offen sc1 lds\n\t"
      "v_exp_f32 v64, v64\n\t"
      "v_exp_f32 v65, v65\n\t"
      "v_smfmac_f32_32x32x32_bf16 v[16:31], v[144:147], v[88:95], %[idx]\n\t"
      "ds_read_b128 v[144:147], v177 offset:512\n\t"
      "v_exp_f32 v66, v66\n\t"
      "v_exp_f32 v67, v67\n\t"
      "v_exp_f32 v68, v68\n\t"
      "v_mfma_f32_32x32x16_bf16 v[80:95], v[136:139], v[132:135], v[48:63]\n\t"
      "ds_read_b128 v[136:139], v176 offset:1024\n\t"
      "v_exp_f32 v69, v69\n\t"
      "v_exp_f32 v70, v70\n\t"
      "v_exp_f32 v71, v71\n\t"
      "v_exp_f32 v72, v72\n\t"
      "v_exp_f32 v73, v73\n\t"
      "s_waitcnt lgkmcnt(2)\n\t"
      "v_smfmac_f32_32x32x32_bf16 v[0:15], v[140:143], v[64:71], %[idx]\n\t"
      "v_exp_f32 v74, v74\n\t"
      "v_exp_f32 v75, v75\n\t"
      "v_exp_f32 v76, v76\n\t"
      "v_exp_f32 v77, v77\n\t"
      "v_exp_f32 v78, v78\n\t"
      "v_exp_f32 v79, v79\n\t"
      "s_setprio 2\n\t"
      "s_add_u32 %[t1], %[gr], 1\n\t"
      "s_cmp_ge_u32 %[t1], 7\n\t"
      "s_cselect_b32 vcc_lo, 7, 0\n\t"
      "s_sub_u32 %[t1], %[t1], vcc_lo\n\t"
      "s_lshl_b32 %[t1], %[t1], 16\n\t"
      "s_add_u32 s48, s44, %[t1]\n\t"
      "s_addc_u32 s49, s45, 0\n\t"
      "s_add_u32 m0, %[l4], 65536\n\t"
      "s_nop 0\n\t"
      "global_load_lds_dwordx4 v172, s[48:49]\n\t"
      "v_exp_f32 v80, v80\n\t"
      "v_exp_f32 v81, v81\n\t"
      "s_waitcnt lgkmcnt(0)\n\t"
      "v_smfmac_f32_32x32x32_bf16 v[0:15], v[144:147], v[72:79], %[idx]\n\t"
      "v_exp_f32 v82, v82\n\t"
      "v_exp_f32 v83, v83\n\t"
      "v_exp_f32 v84, v84\n\t"
      "v_mfma_f32_32x32x16_bf16 v[64:79], v[136:139], v[128:131], v[32:47]\n\t"
      "v_exp_f32 v85, v85\n\t"
      "v_exp_f32 v86, v86\n\t"
      "v_exp_f32 v87, v87\n\t"
      "v_exp_f32 v88, v88\n\t"
      "v_exp_f32 v89, v89\n\t"
      "v_smfmac_f32_32x32x32_bf16 v[16:31], v[140:143], v[80:87], %[idx]\n\t"
      "ds_read_b128 v[140:143], v177 offset:1024\n\t"
      "v_exp_f32 v90, v90\n\t"
      "v_exp_f32 v91, v91\n\t"
      "v_exp_f32 v92, v92\n\t"
      "v_exp_f32 v93, v93\n\t"
      "v_exp_f32 v94, v94\n\t"
      "v_exp_f32 v95, v95\n\t"
      "buffer_load_dword v186, v183, %[rs], 0 offen sc1\n\t"
      "v_exp_f32 v64, v64\n\t"
      "v_exp_f32 v65, v65\n\t"
      "v_smfmac_f32_32x32x32_bf16 v[16:31], v[144:147], v[88:95], %[idx]\n\t"
      "ds_read_b128 v[144:147], v177 offset:1536\n\t"
      "v_exp_f32 v66, v66\n\t"
      "v_exp_f32 v67, v67\n\t"
      "v_exp_f32 v68, v68\n\t"
      "v_mfma_f32_32x32x16_bf16 v[80:95], v[136:139], v[132:135], v[48:63]\n\t"
      "ds_read_b128 v[136:139], v176 offset:2048\n\t"
      "v_exp_f32 v69, v69\n\t"
      "v_exp_f32 v70, v70\n\t"
      "v_exp_f32 v71, v71\n\t"
      "v_exp_f32 v72, v72\n\t"
      "v_exp_f32 v73, v73\n\t"
      "s_waitcnt lgkmcnt(2)\n\t"
      "v_smfmac_f32_32x32x32_bf16 v[0:15], v[140:143], v[64:71], %[idx]\n\t"
      "v_exp_f32 v74, v74\n\t"
      "v_exp_f32 v75, v75\n\t"
      "v_exp_f32 v76, v76\n\t"
      "v_exp_f32 v77, v77\n\t"
      "v_exp_f32 v78, v78\n\t"
      "v_exp_f32 v79, v79\n\t"
      "s_setprio 1\n\t"
      "s_add_u32 m0, %[l4], 66560\n\t"
      "s_nop 0\n\t"
      "global_load_lds_dwordx4 v173, s[48:49]\n\t"
      "v_exp_f32 v80, v80\n\t"
      "v_exp_f32 v81, v81\n\t"
      "s_waitcnt lgkmcnt(0)\n\t"
      "v_smfmac_f32_32x32x32_bf16 v[0:15], v[144:147], v[72:79], %[idx]\n\t"
      "v_exp_f32 v82, v82\n\t"
      "v_exp_f32 v83, v83\n\t"
      "v_exp_f32 v84, v84\n\t"
      "v_mfma_f32_32x32x16_bf16 v[64:79], v[136:139], v[128:131], v[32:47]\n\t"
      "v_exp_f32 v85, v85\n\t"
      "v_exp_f32 v86, v86\n\t"
      "v_exp_f32 v87, v87\n\t"
      "v_exp_f32 v88, v88\n\t"
      "v_exp_f32 v89, v89\n\t"
      "v_smfmac_f32_32x32x32_bf16 v[16:31], v[140:143], v[80:87], %[idx]\n\t"
      "ds_read_b128 v[140:143], v177 offset:2048\n\t"
      "v_exp_f32 v90, v90\n\t"
      "v_exp_f32 v91, v91\n\t"
      "v_exp_f32 v92, v92\n\t"
      "v_exp_f32 v93, v93\n\t"
      "v_exp_f32 v94, v94\n\t"
      "v_exp_f32 v95, v95\n\t"
      "s_add_u32 m0, %[l4], 67584\n\t"
      "s_nop 0\n\t"
      "global_load_lds_dwordx4 v174, s[48:49]\n\t"
      "v_exp_f32 v64, v64\n\t"
      "v_exp_f32 v65, v65\n\t"
      "v_smfmac_f32_32x32x32_bf16 v[16:31], v[144:147], v[88:95], %[idx]\n\t"
      "ds_read_b128 v[144:147], v177 offset:2560\n\t"
      "v_exp_f32 v66, v66\n\t"
      "v_exp_f32 v67, v67\n\t"
      "v_exp_f32 v68, v68\n\t"
      "v_mfma_f32_32x32x16_bf16 v[80:95], v[136:139], v[132:135], v[48:63]\n\t"
      "ds_read_b128 v[136:139], v176 offset:3072\n\t"
      "v_exp_f32 v69, v69\n\t"
      "v_exp_f32 v70, v70\n\t"
      "v_exp_f32 v71, v71\n\t"
      "v_exp_f32 v72, v72\n\t"
      "v_exp_f32 v73, v73\n\t"
      "s_waitcnt lgkmcnt(2)\n\t"
      "v_smfmac_f32_32x32x32_bf16 v[0:15], v[140:143], v[64:71], %[idx]\n\t"
      "v_exp_f32 v74, v74\n\t"
      "v_exp_f32 v75, v75\n\t"
      "v_exp_f32 v76, v76\n\t"
      "s_mov_b32 %[t1], 0x10000\n\t"
      "s_waitcnt vmcnt(2)\n\t"
      "SP1_%=:\n\t"
      "v_lshrrev_b32 v187, 8, v186\n\t"
      "v_bfi_b32 v187, 63, v187, v185\n\t"
      "v_cmp_ne_u32 vcc, v187, v185\n\t"
      "v_mov_b32 v185, v187\n\t"
      "s_cbranch_vccz NE1_%=\n\t"
      "buffer_store_dword v187, v184, %[rs], 0 offen sc1\n\t"
      "NE1_%=:\n\t"
      "v_and_b32 v186, v186, v181\n\t"
      "v_cmp_ne_u32 vcc, v186, v182\n\t"
      "s_cbranch_vccz OK1_%=\n\t"
      "s_sleep 2\n\t"
      "buffer_load_dword v186, v183, %[rs], 0 offen sc1\n\t"
      "s_sub_u32 %[t1], %[t1], 1\n\t"
      "s_cmp_lg_u32 %[t1], 0\n\t"
      "s_waitcnt vmcnt(0)\n\t"
      "s_cbranch_scc1 SP1_%=\n\t"
      "OK1_%=:\n\t"
      "s_add_u32 %[t2], %[gr], 0\n\t"
      "s_cmp_ge_u32 %[t2], 7\n\t"
      "s_cselect_b32 vcc_lo, 7, 0\n\t"
      "s_sub_u32 %[t2], %[t2], vcc_lo\n\t"
      "s_lshl_b32 %[t2], %[t2], 12\n\t"
      "s_add_u32 %[t2], %[t2], %[ho]\n\t"
      "buffer_load_dwordx4 v[148:151], %[l16], %[rs], %[t2] offen offset:0 sc1\n\t"
      "v_exp_f32 v77, v77\n\t"
      "v_exp_f32 v78, v78\n\t"
      "v_exp_f32 v79, v79\n\t"
      "s_setprio 0\n\t"
      "s_add_u32 m0, %[l4], 68608\n\t"
      "s_nop 0\n\t"
      "global_load_lds_dwordx4 v175, s[48:49]\n\t"
      "buffer_load_dwordx4 v[152:155], %[l16], %[rs], %[t2] offen offset:1024 sc1\n\t"
      "v_exp_f32 v80, v80\n\t"
      "v_exp_f32 v81, v81\n\t"
      "s_waitcnt lgkmcnt(0)\n\t"
      "v_smfmac_f32_32x32x32_bf16 v[0:15], v[144:147], v[72:79], %[idx]\n\t"
      "v_exp_f32 v82, v82\n\t"
      "v_exp_f32 v83, v83\n\t"
      "v_exp_f32 v84, v84\n\t"
      "v_mfma_f32_32x32x16_bf16 v[64:79], v[136:139], v[128:131], v[32:47]\n\t"
      "v_exp_f32 v85, v85\n\t"
      "v_exp_f32 v86, v86\n\t"
      "v_exp_f32 v87, v87\n\t"
      "v_exp_f32 v88, v88\n\t"
      "v_exp_f32 v89, v89\n\t"
      "v_smfmac_f32_32x32x32_bf16 v[16:31], v[140:143], v[80:87], %[idx]\n\t"
      "ds_read_b128 v[140:143], v177 offset:3072\n\t"
      "v_exp_f32 v90, v90\n\t"
      "v_exp_f32 v91, v91\n\t"
      "v_exp_f32 v92, v92\n\t"
      "buffer_load_dwordx4 v[156:159], %[l16], %[rs], %[t2] offen offset:2048 sc1\n\t"
      "v_exp_f32 v93, v93\n\t"
      "v_exp_f32 v94, v94\n\t"
      "v_exp_f32 v95, v95\n\t"
      "buffer_load_dwordx4 v[160:163], %[l16], %[rs], %[t2] offen offset:3072 sc1\n\t"
      "v_exp_f32 v64, v64\n\t"
      "v_exp_f32 v65, v65\n\t"
      "v_smfmac_f32_32x32x32_bf16 v[16:31], v[144:147], v[88:95], %[idx]\n\t"
      "ds_read_b128 v[144:147], v177 offset:3584\n\t"
      "v_exp_f32 v66, v66\n\t"
      "v_exp_f32 v67, v67\n\t"
      "v_exp_f32 v68, v68\n\t"
      "v_mfma_f32_32x32x16_bf16 v[80:95], v[136:139], v[132:135], v[48:63]\n\t"
      "s_waitcnt vmcnt(9)\n\t"
      "s_barrier\n\t"
      "s_setprio 3\n\t"
      "v_add_u32 v176, 4294868992, v176\n\t"
      "ds_read_b128 v[136:139], v176 offset:0\n\t"
      "v_exp_f32 v69, v69\n\t"
      "v_exp_f32 v70, v70\n\t"
      "v_exp_f32 v71, v71\n\t"
      "v_exp_f32 v72, v72\n\t"
      "v_exp_f32 v73, v73\n\t"
      "s_waitcnt lgkmcnt(2)\n\t"
      "v_smfmac_f32_32x32x32_bf16 v[0:15], v[140:143], v[64:71], %[idx]\n\t"
      "v_exp_f32 v74, v74\n\t"
      "v_exp_f32 v75, v75\n\t"
      "v_exp_f32 v76, v76\n\t"
      "ds_read_b64_tr_b16 v[164:165], v178 offset:0\n\t"
      "ds_read_b64_tr_b16 v[166:167], v178 offset:1024\n\t"
      "v_exp_f32 v77, v77\n\t"
      "v_exp_f32 v78, v78\n\t"
      "v_exp_f32 v79, v79\n\t"
      "s_add_u32 m0, %[l1], 49152\n\t"
      "s_add_u32 %[t1], %[ko], 32768\n\t"
      "buffer_load_dwordx4 %[l16], %[rs], %[t1] offen sc1 lds\n\t"
      "v_exp_f32 v80, v80\n\t"
      "v_exp_f32 v81, v81\n\t"
      "s_waitcnt lgkmcnt(2)\n\t"
      "v_smfmac_f32_32x32x32_bf16 v[0:15], v[144:147], v[72:79], %[idx]\n\t"
      "v_exp_f32 v82, v82\n\t"
      "v_exp_f32 v83, v83\n\t"
      "v_exp_f32 v84, v84\n\t"
      "v_mfma_f32_32x32x16_bf16 v[64:79], v[136:139], v[128:131], v[32:47]\n\t"
      "v_exp_f32 v85, v85\n\t"
      "v_exp_f32 v86, v86\n\t"
      "v_exp_f32 v87, v87\n\t"
      "v_exp_f32 v88, v88\n\t"
      "v_exp_f32 v89, v89\n\t"
      "v_smfmac_f32_32x32x32_bf16 v[16:31], v[140:143], v[80:87], %[idx]\n\t"
      "s_mov_b32 %[t1], 4294868992\n\t"
      "v_mad_i32_i24 v177, %[t1], v180, v177\n\t"
      "ds_read_b128 v[140:143], v177 offset:0\n\t"
      "v_exp_f32 v90, v90\n\t"
      "v_exp_f32 v91, v91\n\t"
      "v_exp_f32 v92, v92\n\t"
      "ds_read_b64_tr_b16 v[168:169], v179 offset:0\n\t"
      "ds_read_b64_tr_b16 v[170:171], v179 offset:1024\n\t"
      "v_exp_f32 v93, v93\n\t"
      "v_exp_f32 v94, v94\n\t"
      "v_exp_f32 v95, v95\n\t"
      "s_add_u32 m0, %[l1], 57344\n\t"
      "s_add_u32 %[t1], %[ko], 2129920\n\t"
      "buffer_load_dwordx4 %[l16], %[rs], %[t1] offen sc1 lds\n\t"
      "v_exp_f32 v64, v64\n\t"
      "v_exp_f32 v65, v65\n\t"
      "v_smfmac_f32_32x32x32_bf16 v[16:31], v[144:147], v[88:95], %[idx]\n\t"
      "ds_read_b128 v[144:147], v177 offset:512\n\t"
      "v_exp_f32 v66, v66\n\t"
      "v_exp_f32 v67, v67\n\t"
      "v_exp_f32 v68, v68\n\t"
      "v_mfma_f32_32x32x16_bf16 v[80:95], v[136:139], v[132:135], v[48:63]\n\t"
      "ds_read_b128 v[136:139], v176 offset:1024\n\t"
      "v_exp_f32 v69, v69\n\t"
      "v_exp_f32 v70, v70\n\t"
      "v_exp_f32 v71, v71\n\t"
      "v_exp_f32 v72, v72\n\t"
      "v_exp_f32 v73, v73\n\t"
      "s_waitcnt lgkmcnt(4)\n\t"
      "v_smfmac_f32_32x32x32_bf16 v[0:15], v[140:143], v[64:71], %[idx]\n\t"
      "v_exp_f32 v74, v74\n\t"
      "v_exp_f32 v75, v75\n\t"
      "v_exp_f32 v76, v76\n\t"
      "s_waitcnt vmcnt(4)\n\t"
      "v_mfma_f32_32x32x16_bf16 v[96:111], v[164:167], v[148:151], 0\n\t"
      "ds_read_b64_tr_b16 v[164:165], v178 offset:2048\n\t"
      "ds_read_b64_tr_b16 v[166:167], v178 offset:3072\n\t"
      "v_exp_f32 v77, v77\n\t"
      "v_exp_f32 v78, v78\n\t"
      "v_exp_f32 v79, v79\n\t"
      "s_setprio 2\n\t"
      "s_add_u32 %[t1], %[gr], 2\n\t"
      "s_cmp_ge_u32 %[t1], 7\n\t"
      "s_cselect_b32 vcc_lo, 7, 0\n\t"
      "s_sub_u32 %[t1], %[t1], vcc_lo\n\t"
      "s_lshl_b32 %[t1], %[t1], 16\n\t"
      "s_add_u32 s48, s44, %[t1]\n\t"
      "s_addc_u32 s49, s45, 0\n\t"
      "s_add_u32 m0, %[l4], 114688\n\t"
      "s_nop 0\n\t"
      "global_load_lds_dwordx4 v172, s[48:49]\n\t"
      "v_exp_f32 v80, v80\n\t"
      "v_exp_f32 v81, v81\n\t"
      "s_waitcnt lgkmcnt(2)\n\t"
      "v_smfmac_f32_32x32x32_bf16 v[0:15], v[144:147], v[72:79], %[idx]\n\t"
      "v_exp_f32 v82, v82\n\t"
      "v_exp_f32 v83, v83\n\t"
      "v_exp_f32 v84, v84\n\t"
      "v_mfma_f32_32x32x16_bf16 v[64:79], v[136:139], v[128:131], v[32:47]\n\t"
      "v_exp_f32 v85, v85\n\t"
      "v_exp_f32 v86, v86\n\t"
      "v_exp_f32 v87, v87\n\t"
      "v_exp_f32 v88, v88\n\t"
      "v_exp_f32 v89, v89\n\t"
      "v_smfmac_f32_32x32x32_bf16 v[16:31], v[140:143], v[80:87], %[idx]\n\t"
      "ds_read_b128 v[140:143], v177 offset:1024\n\t"
      "v_exp_f32 v90, v90\n\t"
      "v_exp_f32 v91, v91\n\t"
      "v_exp_f32 v92, v92\n\t"
      "v_mfma_f32_32x32x16_bf16 v[112:127], v[168:171], v[148:151], 0\n\t"
      "ds_read_b64_tr_b16 v[168:169], v179 offset:2048\n\t"
      "ds_read_b64_tr_b16 v[170:171], v179 offset:3072\n\t"
      "s_add_u32 %[t2], %[gr], 1\n\t"
      "s_cmp_ge_u32 %[t2], 7\n\t"
      "s_cselect_b32 vcc_lo, 7, 0\n\t"
      "s_sub_u32 %[t2], %[t2], vcc_lo\n\t"
      "s_lshl_b32 %[t2], %[t2], 12\n\t"
      "s_add_u32 %[t2], %[t2], %[ho]\n\t"
      "buffer_load_dwordx4 v[148:151], %[l16], %[rs], %[t2] offen offset:0 sc1\n\t"
      "v_exp_f32 v93, v93\n\t"
      "v_exp_f32 v94, v94\n\t"
      "v_exp_f32 v95, v95\n\t"
      "v_exp_f32 v64, v64\n\t"
      "v_exp_f32 v65, v65\n\t"
      "v_smfmac_f32_32x32x32_bf16 v[16:31], v[144:147], v[88:95], %[idx]\n\t"
      "ds_read_b128 v[144:147], v177 offset:1536\n\t"
      "v_exp_f32 v66, v66\n\t"
      "v_exp_f32 v67, v67\n\t"
      "v_exp_f32 v68, v68\n\t"
      "v_mfma_f32_32x32x16_bf16 v[80:95], v[136:139], v[132:135], v[48:63]\n\t"
      "ds_read_b128 v[136:139], v176 offset:2048\n\t"
      "v_exp_f32 v69, v69\n\t"
      "v_exp_f32 v70, v70\n\t"
      "v_exp_f32 v71, v71\n\t"
      "v_exp_f32 v72, v72\n\t"
      "v_exp_f32 v73, v73\n\t"
      "s_waitcnt lgkmcnt(4)\n\t"
      "v_smfmac_f32_32x32x32_bf16 v[0:15], v[140:143], v[64:71], %[idx]\n\t"
      "v_exp_f32 v74, v74\n\t"
      "v_exp_f32 v75, v75\n\t"
      "v_exp_f32 v76, v76\n\t"
      "v_mfma_f32_32x32x16_bf16 v[96:111], v[164:167], v[152:155], v[96:111]\n\t"
      "ds_read_b64_tr_b16 v[164:165], v178 offset:4096\n\t"
      "ds_read_b64_tr_b16 v[166:167], v178 offset:5120\n\t"
      "v_exp_f32 v77, v77\n\t"
      "v_exp_f32 v78, v78\n\t"
      "v_exp_f32 v79, v79\n\t"
      "s_setprio 1\n\t"
      "s_add_u32 m0, %[l4], 115712\n\t"
      "s_nop 0\n\t"
      "global_load_lds_dwordx4 v173, s[48:49]\n\t"
      "v_exp_f32 v80, v80\n\t"
      "v_exp_f32 v81, v81\n\t"
      "s_waitcnt lgkmcnt(2)\n\t"
      "v_smfmac_f32_32x32x32_bf16 v[0:15], v[144:147], v[72:79], %[idx]\n\t"
      "v_exp_f32 v82, v82\n\t"
      "v_exp_f32 v83, v83\n\t"
      "v_exp_f32 v84, v84\n\t"
      "v_mfma_f32_32x32x16_bf16 v[64:79], v[136:139], v[128:131], v[32:47]\n\t"
      "v_exp_f32 v85, v85\n\t"
      "v_exp_f32 v86, v86\n\t"
      "v_exp_f32 v87, v87\n\t"
      "v_exp_f32 v88, v88\n\t"
      "v_exp_f32 v89, v89\n\t"
      "v_smfmac_f32_32x32x32_bf16 v[16:31], v[140:143], v[80:87], %[idx]\n\t"
      "ds_read_b128 v[140:143], v177 offset:2048\n\t"
      "v_exp_f32 v90, v90\n\t"
      "v_exp_f32 v91, v91\n\t"
      "v_exp_f32 v92, v92\n\t"
      "v_mfma_f32_32x32x16_bf16 v[112:127], v[168:171], v[152:155], v[112:127]\n\t"
      "ds_read_b64_tr_b16 v[168:169], v179 offset:4096\n\t"
      "ds_read_b64_tr_b16 v[170:171], v179 offset:5120\n\t"
      "buffer_load_dwordx4 v[152:155], %[l16], %[rs], %[t2] offen offset:1024 sc1\n\t"
      "v_exp_f32 v93, v93\n\t"
      "v_exp_f32 v94, v94\n\t"
      "v_exp_f32 v95, v95\n\t"
      "s_add_u32 m0, %[l4], 116736\n\t"
      "s_nop 0\n\t"
      "global_load_lds_dwordx4 v174, s[48:49]\n\t"
      "v_exp_f32 v64, v64\n\t"
      "v_exp_f32 v65, v65\n\t"
      "v_smfmac_f32_32x32x32_bf16 v[16:31], v[144:147], v[88:95], %[idx]\n\t"
      "ds_read_b128 v[144:147], v177 offset:2560\n\t"
      "v_exp_f32 v66, v66\n\t"
      "v_exp_f32 v67, v67\n\t"
      "v_exp_f32 v68, v68\n\t"
      "v_mfma_f32_32x32x16_bf16 v[80:95], v[136:139], v[132:135], v[48:63]\n\t"
      "ds_read_b128 v[136:139], v176 offset:3072\n\t"
      "v_exp_f32 v69, v69\n\t"
      "v_exp_f32 v70, v70\n\t"
      "v_exp_f32 v71, v71\n\t"
      "v_exp_f32 v72, v72\n\t"
      "v_exp_f32 v73, v73\n\t"
      "s_waitcnt lgkmcnt(4)\n\t"
      "v_smfmac_f32_32x32x32_bf16 v[0:15], v[140:143], v[64:71], %[idx]\n\t"
      "v_exp_f32 v74, v74\n\t"
      "v_exp_f32 v75, v75\n\t"
      "v_exp_f32 v76, v76\n\t"
      "s_waitcnt vmcnt(7)\n\t"
      "v_mfma_f32_32x32x16_bf16 v[96:111], v[164:167], v[156:159], v[96:111]\n\t"
      "ds_read_b64_tr_b16 v[164:165], v178 offset:6144\n\t"
      "ds_read_b64_tr_b16 v[166:167], v178 offset:7168\n\t"
      "v_exp_f32 v77, v77\n\t"
      "v_exp_f32 v78, v78\n\t"
      "v_exp_f32 v79, v79\n\t"
      "s_setprio 0\n\t"
      "s_add_u32 m0, %[l4], 117760\n\t"
      "s_nop 0\n\t"
      "global_load_lds_dwordx4 v175, s[48:49]\n\t"
      "v_exp_f32 v80, v80\n\t"
      "v_exp_f32 v81, v81\n\t"
      "s_waitcnt lgkmcnt(2)\n\t"
      "v_smfmac_f32_32x32x32_bf16 v[0:15], v[144:147], v[72:79], %[idx]\n\t"
      "v_exp_f32 v82, v82\n\t"
      "v_exp_f32 v83, v83\n\t"
      "v_exp_f32 v84, v84\n\t"
      "v_mfma_f32_32x32x16_bf16 v[64:79], v[136:139], v[128:131], v[32:47]\n\t"
      "v_exp_f32 v85, v85\n\t"
      "v_exp_f32 v86, v86\n\t"
      "v_exp_f32 v87, v87\n\t"
      "v_exp_f32 v88, v88\n\t"
      "v_exp_f32 v89, v89\n\t"
      "v_smfmac_f32_32x32x32_bf16 v[16:31], v[140:143], v[80:87], %[idx]\n\t"
      "ds_read_b128 v[140:143], v177 offset:3072\n\t"
      "v_exp_f32 v90, v90\n\t"
      "v_exp_f32 v91, v91\n\t"
      "v_exp_f32 v92, v92\n\t"
      "v_mfma_f32_32x32x16_bf16 v[112:127], v[168:171], v[156:159], v[112:127]\n\t"
      "ds_read_b64_tr_b16 v[168:169], v179 offset:6144\n\t"
      "ds_read_b64_tr_b16 v[170:171], v179 offset:7168\n\t"
      "buffer_load_dwordx4 v[156:159], %[l16], %[rs], %[t2] offen offset:2048 sc1\n\t"
      "v_exp_f32 v93, v93\n\t"
      "v_exp_f32 v94, v94\n\t"
      "v_exp_f32 v95, v95\n\t"
      "v_exp_f32 v64, v64\n\t"
      "v_exp_f32 v65, v65\n\t"
      "v_smfmac_f32_32x32x32_bf16 v[16:31], v[144:147], v[88:95], %[idx]\n\t"
      "ds_read_b128 v[144:147], v177 offset:3584\n\t"
      "v_exp_f32 v66, v66\n\t"
      "v_exp_f32 v67, v67\n\t"
      "v_exp_f32 v68, v68\n\t"
      "v_mfma_f32_32x32x16_bf16 v[80:95], v[136:139], v[132:135], v[48:63]\n\t"
      "s_waitcnt vmcnt(7) lgkmcnt(1)\n\t"
      "s_barrier\n\t"
      "s_setprio 3\n\t"
      "v_add_u32 v178, 49152, v178\n\t"
      "v_add_u32 v179, 49152, v179\n\t"
      "v_add_u32 v176, 49152, v176\n\t"
      "ds_read_b128 v[136:139], v176 offset:0\n\t"
      "v_exp_f32 v69, v69\n\t"
      "v_exp_f32 v70, v70\n\t"
      "v_exp_f32 v71, v71\n\t"
      "v_exp_f32 v72, v72\n\t"
      "v_exp_f32 v73, v73\n\t"
      "v_smfmac_f32_32x32x32_bf16 v[0:15], v[140:143], v[64:71], %[idx]\n\t"
      "v_exp_f32 v74, v74\n\t"
      "v_exp_f32 v75, v75\n\t"
      "v_exp_f32 v76, v76\n\t"
      "v_mfma_f32_32x32x16_bf16 v[96:111], v[164:167], v[160:163], v[96:111]\n\t"
      "ds_read_b64_tr_b16 v[164:165], v178 offset:0\n\t"
      "ds_read_b64_tr_b16 v[166:167], v178 offset:1024\n\t"
      "v_exp_f32 v77, v77\n\t"
      "v_exp_f32 v78, v78\n\t"
      "v_exp_f32 v79, v79\n\t"
      "s_add_u32 m0, %[l1], 98304\n\t"
      "s_add_u32 %[t1], %[ko], 40960\n\t"
      "buffer_load_dwordx4 %[l16], %[rs], %[t1] offen sc1 lds\n\t"
      "v_exp_f32 v80, v80\n\t"
      "v_exp_f32 v81, v81\n\t"
      "s_waitcnt lgkmcnt(2)\n\t"
      "v_smfmac_f32_32x32x32_bf16 v[0:15], v[144:147], v[72:79], %[idx]\n\t"
      "v_exp_f32 v82, v82\n\t"
      "v_exp_f32 v83, v83\n\t"
      "v_exp_f32 v84, v84\n\t"
      "v_mfma_f32_32x32x16_bf16 v[64:79], v[136:139], v[128:131], v[32:47]\n\t"
      "v_exp_f32 v85, v85\n\t"
      "v_exp_f32 v86, v86\n\t"
      "v_exp_f32 v87, v87\n\t"
      "v_exp_f32 v88, v88\n\t"
      "v_exp_f32 v89, v89\n\t"
      "v_smfmac_f32_32x32x32_bf16 v[16:31], v[140:143], v[80:87], %[idx]\n\t"
      "s_mov_b32 %[t1], 49152\n\t"
      "v_mad_i32_i24 v177, %[t1], v180, v177\n\t"
      "ds_read_b128 v[140:143], v177 offset:0\n\t"
      "v_exp_f32 v90, v90\n\t"
      "v_exp_f32 v91, v91\n\t"
      "v_exp_f32 v92, v92\n\t"
      "v_mfma_f32_32x32x16_bf16 v[112:127], v[168:171], v[160:163], v[112:127]\n\t"
      "ds_read_b64_tr_b16 v[168:169], v179 offset:0\n\t"
      "ds_read_b64_tr_b16 v[170:171], v179 offset:1024\n\t"
      "buffer_load_dwordx4 v[160:163], %[l16], %[rs], %[t2] offen offset:3072 sc1\n\t"
      "v_exp_f32 v93, v93\n\t"
      "v_exp_f32 v94, v94\n\t"
      "v_exp_f32 v95, v95\n\t"
      "s_add_u32 m0, %[l1], 106496\n\t"
      "s_add_u32 %[t1], %[ko], 2138112\n\t"
      "buffer_load_dwordx4 %[l16], %[rs], %[t1] offen sc1 lds\n\t"
      "v_exp_f32 v64, v64\n\t"
      "v_exp_f32 v65, v65\n\t"
      "v_smfmac_f32_32x32x32_bf16 v[16:31], v[144:147], v[88:95], %[idx]\n\t"
      "ds_read_b128 v[144:147], v177 offset:512\n\t"
      "v_exp_f32 v66, v66\n\t"
      "v_exp_f32 v67, v67\n\t"
      "v_exp_f32 v68, v68\n\t"
      "v_mfma_f32_32x32x16_bf16 v[80:95], v[136:139], v[132:135], v[48:63]\n\t"
      "ds_read_b128 v[136:139], v176 offset:1024\n\t"
      "v_exp_f32 v69, v69\n\t"
      "v_exp_f32 v70, v70\n\t"
      "v_exp_f32 v71, v71\n\t"
      "v_exp_f32 v72, v72\n\t"
      "v_exp_f32 v73, v73\n\t"
      "s_waitcnt lgkmcnt(4)\n\t"
      "v_smfmac_f32_32x32x32_bf16 v[0:15], v[140:143], v[64:71], %[idx]\n\t"
      "v_exp_f32 v74, v74\n\t"
      "v_exp_f32 v75, v75\n\t"
      "v_exp_f32 v76, v76\n\t"
      "s_waitcnt vmcnt(6)\n\t"
      "v_mfma_f32_32x32x16_bf16 v[96:111], v[164:167], v[148:151], v[96:111]\n\t"
      "ds_read_b64_tr_b16 v[164:165], v178 offset:2048\n\t"
      "ds_read_b64_tr_b16 v[166:167], v178 offset:3072\n\t"
      "v_exp_f32 v77, v77\n\t"
      "v_exp_f32 v78, v78\n\t"
      "v_exp_f32 v79, v79\n\t"
      "s_setprio 2\n\t"
      "s_add_u32 %[t1], %[gr], 3\n\t"
      "s_cmp_ge_u32 %[t1], 7\n\t"
      "s_cselect_b32 vcc_lo, 7, 0\n\t"
      "s_sub_u32 %[t1], %[t1], vcc_lo\n\t"
      "s_lshl_b32 %[t1], %[t1], 16\n\t"
      "s_add_u32 s48, s44, %[t1]\n\t"
      "s_addc_u32 s49, s45, 0\n\t"
      "s_add_u32 m0, %[l4], 16384\n\t"
      "s_nop 0\n\t"
      "global_load_lds_dwordx4 v172, s[48:49]\n\t"
      "v_exp_f32 v80, v80\n\t"
      "v_exp_f32 v81, v81\n\t"
      "s_waitcnt lgkmcnt(2)\n\t"
      "v_smfmac_f32_32x32x32_bf16 v[0:15], v[144:147], v[72:79], %[idx]\n\t"
      "v_exp_f32 v82, v82\n\t"
      "v_exp_f32 v83, v83\n\t"
      "v_exp_f32 v84, v84\n\t"
      "v_mfma_f32_32x32x16_bf16 v[64:79], v[136:139], v[128:131], v[32:47]\n\t"
      "v_exp_f32 v85, v85\n\t"
      "v_exp_f32 v86, v86\n\t"
      "v_exp_f32 v87, v87\n\t"
      "v_exp_f32 v88, v88\n\t"
      "v_exp_f32 v89, v89\n\t"
      "v_smfmac_f32_32x32x32_bf16 v[16:31], v[140:143], v[80:87], %[idx]\n\t"
      "ds_read_b128 v[140:143], v177 offset:1024\n\t"
      "v_exp_f32 v90, v90\n\t"
      "v_exp_f32 v91, v91\n\t"
      "v_exp_f32 v92, v92\n\t"
      "v_mfma_f32_32x32x16_bf16 v[112:127], v[168:171], v[148:151], v[112:127]\n\t"
      "ds_read_b64_tr_b16 v[168:169], v179 offset:2048\n\t"
      "ds_read_b64_tr_b16 v[170:171], v179 offset:3072\n\t"
      "s_add_u32 %[t2], %[gr], 2\n\t"
      "s_cmp_ge_u32 %[t2], 7\n\t"
      "s_cselect_b32 vcc_lo, 7, 0\n\t"
      "s_sub_u32 %[t2], %[t2], vcc_lo\n\t"
      "s_lshl_b32 %[t2], %[t2], 12\n\t"
      "s_add_u32 %[t2], %[t2], %[ho]\n\t"
      "buffer_load_dwordx4 v[148:151], %[l16], %[rs], %[t2] offen offset:0 sc1\n\t"
      "v_exp_f32 v93, v93\n\t"
      "v_exp_f32 v94, v94\n\t"
      "v_exp_f32 v95, v95\n\t"
      "v_exp_f32 v64, v64\n\t"
      "v_exp_f32 v65, v65\n\t"
      "v_smfmac_f32_32x32x32_bf16 v[16:31], v[144:147], v[88:95], %[idx]\n\t"
      "ds_read_b128 v[144:147], v177 offset:1536\n\t"
      "v_exp_f32 v66, v66\n\t"
      "v_exp_f32 v67, v67\n\t"
      "v_exp_f32 v68, v68\n\t"
      "v_mfma_f32_32x32x16_bf16 v[80:95], v[136:139], v[132:135], v[48:63]\n\t"
      "ds_read_b128 v[136:139], v176 offset:2048\n\t"
      "v_exp_f32 v69, v69\n\t"
      "v_exp_f32 v70, v70\n\t"
      "v_exp_f32 v71, v71\n\t"
      "v_exp_f32 v72, v72\n\t"
      "v_exp_f32 v73, v73\n\t"
      "s_waitcnt lgkmcnt(4)\n\t"
      "v_smfmac_f32_32x32x32_bf16 v[0:15], v[140:143], v[64:71], %[idx]\n\t"
      "v_exp_f32 v74, v74\n\t"
      "v_exp_f32 v75, v75\n\t"
      "v_exp_f32 v76, v76\n\t"
      "v_mfma_f32_32x32x16_bf16 v[96:111], v[164:167], v[152:155], v[96:111]\n\t"
      "ds_read_b64_tr_b16 v[164:165], v178 offset:4096\n\t"
      "ds_read_b64_tr_b16 v[166:167], v178 offset:5120\n\t"
      "v_exp_f32 v77, v77\n\t"
      "v_exp_f32 v78, v78\n\t"
      "v_exp_f32 v79, v79\n\t"
      "s_setprio 1\n\t"
      "s_add_u32 m0, %[l4], 17408\n\t"
      "s_nop 0\n\t"
      "global_load_lds_dwordx4 v173, s[48:49]\n\t"
      "v_exp_f32 v80, v80\n\t"
      "v_exp_f32 v81, v81\n\t"
      "s_waitcnt lgkmcnt(2)\n\t"
      "v_smfmac_f32_32x32x32_bf16 v[0:15], v[144:147], v[72:79], %[idx]\n\t"
      "v_exp_f32 v82, v82\n\t"
      "v_exp_f32 v83, v83\n\t"
      "v_exp_f32 v84, v84\n\t"
      "v_mfma_f32_32x32x16_bf16 v[64:79], v[136:139], v[128:131], v[32:47]\n\t"
      "v_exp_f32 v85, v85\n\t"
      "v_exp_f32 v86, v86\n\t"
      "v_exp_f32 v87, v87\n\t"
      "v_exp_f32 v88, v88\n\t"
      "v_exp_f32 v89, v89\n\t"
      "v_smfmac_f32_32x32x32_bf16 v[16:31], v[140:143], v[80:87], %[idx]\n\t"
      "ds_read_b128 v[140:143], v177 offset:2048\n\t"
      "v_exp_f32 v90, v90\n\t"
      "v_exp_f32 v91, v91\n\t"
      "v_exp_f32 v92, v92\n\t"
      "v_mfma_f32_32x32x16_bf16 v[112:127], v[168:171], v[152:155], v[112:127]\n\t"
      "ds_read_b64_tr_b16 v[168:169], v179 offset:4096\n\t"
      "ds_read_b64_tr_b16 v[170:171], v179 offset:5120\n\t"
      "buffer_load_dwordx4 v[152:155], %[l16], %[rs], %[t2] offen offset:1024 sc1\n\t"
      "v_exp_f32 v93, v93\n\t"
      "v_exp_f32 v94, v94\n\t"
      "v_exp_f32 v95, v95\n\t"
      "s_add_u32 m0, %[l4], 18432\n\t"
      "s_nop 0\n\t"
      "global_load_lds_dwordx4 v174, s[48:49]\n\t"
      "v_exp_f32 v64, v64\n\t"
      "v_exp_f32 v65, v65\n\t"
      "v_smfmac_f32_32x32x32_bf16 v[16:31], v[144:147], v[88:95], %[idx]\n\t"
      "ds_read_b128 v[144:147], v177 offset:2560\n\t"
      "v_exp_f32 v66, v66\n\t"
      "v_exp_f32 v67, v67\n\t"
      "v_exp_f32 v68, v68\n\t"
      "v_mfma_f32_32x32x16_bf16 v[80:95], v[136:139], v[132:135], v[48:63]\n\t"
      "ds_read_b128 v[136:139], v176 offset:3072\n\t"
      "v_exp_f32 v69, v69\n\t"
      "v_exp_f32 v70, v70\n\t"
      "v_exp_f32 v71, v71\n\t"
      "v_exp_f32 v72, v72\n\t"
      "v_exp_f32 v73, v73\n\t"
      "s_waitcnt lgkmcnt(4)\n\t"
      "v_smfmac_f32_32x32x32_bf16 v[0:15], v[140:143], v[64:71], %[idx]\n\t"
      "v_exp_f32 v74, v74\n\t"
      "v_exp_f32 v75, v75\n\t"
      "v_exp_f32 v76, v76\n\t"
      "s_waitcnt vmcnt(6)\n\t"
      "v_mfma_f32_32x32x16_bf16 v[96:111], v[164:167], v[156:159], v[96:111]\n\t"
      "ds_read_b64_tr_b16 v[164:165], v178 offset:6144\n\t"
      "ds_read_b64_tr_b16 v[166:167], v178 offset:7168\n\t"
      "v_exp_f32 v77, v77\n\t"
      "v_exp_f32 v78, v78\n\t"
      "v_exp_f32 v79, v79\n\t"
      "s_setprio 0\n\t"
      "s_add_u32 m0, %[l4], 19456\n\t"
      "s_nop 0\n\t"
      "global_load_lds_dwordx4 v175, s[48:49]\n\t"
      "v_exp_f32 v80, v80\n\t"
      "v_exp_f32 v81, v81\n\t"
      "s_waitcnt lgkmcnt(2)\n\t"
      "v_smfmac_f32_32x32x32_bf16 v[0:15], v[144:147], v[72:79], %[idx]\n\t"
      "v_exp_f32 v82, v82\n\t"
      "v_exp_f32 v83, v83\n\t"
      "v_exp_f32 v84, v84\n\t"
      "v_mfma_f32_32x32x16_bf16 v[64:79], v[136:139], v[128:131], v[32:47]\n\t"
      "v_exp_f32 v85, v85\n\t"
      "v_exp_f32 v86, v86\n\t"
      "v_exp_f32 v87, v87\n\t"
      "v_exp_f32 v88, v88\n\t"
      "v_exp_f32 v89, v89\n\t"
      "v_smfmac_f32_32x32x32_bf16 v[16:31], v[140:143], v[80:87], %[idx]\n\t"
      "ds_read_b128 v[140:143], v177 offset:3072\n\t"
      "v_exp_f32 v90, v90\n\t"
      "v_exp_f32 v91, v91\n\t"
      "v_exp_f32 v92, v92\n\t"
      "v_mfma_f32_32x32x16_bf16 v[112:127], v[168:171], v[156:159], v[112:127]\n\t"
      "ds_read_b64_tr_b16 v[168:169], v179 offset:6144\n\t"
      "ds_read_b64_tr_b16 v[170:171], v179 offset:7168\n\t"
      "buffer_load_dwordx4 v[156:159], %[l16], %[rs], %[t2] offen offset:2048 sc1\n\t"
      "v_exp_f32 v93, v93\n\t"
      "v_exp_f32 v94, v94\n\t"
      "v_exp_f32 v95, v95\n\t"
      "s_cmp_lg_u32 %[l1], 0\n\t"
      "s_cbranch_scc1 NT%=\n\t"
      "v_lshlrev_b32 v187, 6, %[l16]\n\t"
      "v_add_u32 v187, %[tw], v187\n\t"
      "s_mov_b32 m0, 160000\n\t"
      "s_sub_u32 s48, s46, %[po]\n\t"
      "s_subb_u32 s49, s47, 0\n\t"
      "global_load_lds_dword v187, s[48:49]\n\t"
      "NT%=:\n\t"
      "v_exp_f32 v64, v64\n\t"
      "v_exp_f32 v65, v65\n\t"
      "v_smfmac_f32_32x32x32_bf16 v[16:31], v[144:147], v[88:95], %[idx]\n\t"
      "ds_read_b128 v[144:147], v177 offset:3584\n\t"
      "v_exp_f32 v66, v66\n\t"
      "v_exp_f32 v67, v67\n\t"
      "v_exp_f32 v68, v68\n\t"
      "v_mfma_f32_32x32x16_bf16 v[80:95], v[136:139], v[132:135], v[48:63]\n\t"
      "s_waitcnt vmcnt(7) lgkmcnt(1)\n\t"
      "s_barrier\n\t"
      "s_setprio 3\n\t"
      "v_add_u32 v178, 49152, v178\n\t"
      "v_add_u32 v179, 49152, v179\n\t"
      "v_add_u32 v176, 49152, v176\n\t"
      "ds_read_b128 v[136:139], v176 offset:0\n\t"
      "v_exp_f32 v69, v69\n\t"
      "v_exp_f32 v70, v70\n\t"
      "v_exp_f32 v71, v71\n\t"
      "v_exp_f32 v72, v72\n\t"
      "v_exp_f32 v73, v73\n\t"
      "v_smfmac_f32_32x32x32_bf16 v[0:15], v[140:143], v[64:71], %[idx]\n\t"
      "v_exp_f32 v74, v74\n\t"
      "v_exp_f32 v75, v75\n\t"
      "v_exp_f32 v76, v76\n\t"
      "v_mfma_f32_32x32x16_bf16 v[96:111], v[164:167], v[160:163], v[96:111]\n\t"
      "ds_read_b64_tr_b16 v[164:165], v178 offset:0\n\t"
      "ds_read_b64_tr_b16 v[166:167], v178 offset:1024\n\t"
      "v_exp_f32 v77, v77\n\t"
      "v_exp_f32 v78, v78\n\t"
      "v_exp_f32 v79, v79\n\t"
      "s_add_u32 m0, %[l1], 0\n\t"
      "s_add_u32 %[t1], %[ko], 49152\n\t"
      "buffer_load_dwordx4 %[l16], %[rs], %[t1] offen sc1 lds\n\t"
      "v_exp_f32 v80, v80\n\t"
      "v_exp_f32 v81, v81\n\t"
      "s_waitcnt lgkmcnt(2)\n\t"
      "v_smfmac_f32_32x32x32_bf16 v[0:15], v[144:147], v[72:79], %[idx]\n\t"
      "v_exp_f32 v82, v82\n\t"
      "v_exp_f32 v83, v83\n\t"
      "v_exp_f32 v84, v84\n\t"
      "v_mfma_f32_32x32x16_bf16 v[64:79], v[136:139], v[128:131], v[32:47]\n\t"
      "v_exp_f32 v85, v85\n\t"
      "v_exp_f32 v86, v86\n\t"
      "v_exp_f32 v87, v87\n\t"
      "v_exp_f32 v88, v88\n\t"
      "v_exp_f32 v89, v89\n\t"
      "v_smfmac_f32_32x32x32_bf16 v[16:31], v[140:143], v[80:87], %[idx]\n\t"
      "s_mov_b32 %[t1], 49152\n\t"
      "v_mad_i32_i24 v177, %[t1], v180, v177\n\t"
      "ds_read_b128 v[140:143], v177 offset:0\n\t"
      "v_exp_f32 v90, v90\n\t"
      "v_exp_f32 v91, v91\n\t"
      "v_exp_f32 v92, v92\n\t"
      "v_mfma_f32_32x32x16_bf16 v[112:127], v[168:171], v[160:163], v[112:127]\n\t"
      "ds_read_b64_tr_b16 v[168:169], v179 offset:0\n\t"
      "ds_read_b64_tr_b16 v[170:171], v179 offset:1024\n\t"
      "buffer_load_dwordx4 v[160:163], %[l16], %[rs], %[t2] offen offset:3072 sc1\n\t"
      "v_exp_f32 v93, v93\n\t"
      "v_exp_f32 v94, v94\n\t"
      "v_exp_f32 v95, v95\n\t"
      "s_add_u32 m0, %[l1], 8192\n\t"
      "s_add_u32 %[t1], %[ko], 2146304\n\t"
      "buffer_load_dwordx4 %[l16], %[rs], %[t1] offen sc1 lds\n\t"
      "v_exp_f32 v64, v64\n\t"
      "v_exp_f32 v65, v65\n\t"
      "v_smfmac_f32_32x32x32_bf16 v[16:31], v[144:147], v[88:95], %[idx]\n\t"
      "ds_read_b128 v[144:147], v177 offset:512\n\t"
      "v_exp_f32 v66, v66\n\t"
      "v_exp_f32 v67, v67\n\t"
      "v_exp_f32 v68, v68\n\t"
      "v_mfma_f32_32x32x16_bf16 v[80:95], v[136:139], v[132:135], v[48:63]\n\t"
      "ds_read_b128 v[136:139], v176 offset:1024\n\t"
      "v_exp_f32 v69, v69\n\t"
      "v_exp_f32 v70, v70\n\t"
      "v_exp_f32 v71, v71\n\t"
      "v_exp_f32 v72, v72\n\t"
      "v_exp_f32 v73, v73\n\t"
      "s_waitcnt lgkmcnt(4)\n\t"
      "v_smfmac_f32_32x32x32_bf16 v[0:15], v[140:143], v[64:71], %[idx]\n\t"
      "v_exp_f32 v74, v74\n\t"
      "v_exp_f32 v75, v75\n\t"
      "v_exp_f32 v76, v76\n\t"
      "s_waitcnt vmcnt(6)\n\t"
      "v_mfma_f32_32x32x16_bf16 v[96:111], v[164:167], v[148:151], v[96:111]\n\t"
      "ds_read_b64_tr_b16 v[164:165], v178 offset:2048\n\t"
      "ds_read_b64_tr_b16 v[166:167], v178 offset:3072\n\t"
      "v_exp_f32 v77, v77\n\t"
      "v_exp_f32 v78, v78\n\t"
      "v_exp_f32 v79, v79\n\t"
      "s_setprio 2\n\t"
      "s_add_u32 %[t1], %[gr], 4\n\t"
      "s_cmp_ge_u32 %[t1], 7\n\t"
      "s_cselect_b32 vcc_lo, 7, 0\n\t"
      "s_sub_u32 %[t1], %[t1], vcc_lo\n\t"
      "s_lshl_b32 %[t1], %[t1], 16\n\t"
      "s_add_u32 s48, s44, %[t1]\n\t"
      "s_addc_u32 s49, s45, 0\n\t"
      "s_add_u32 m0, %[l4], 65536\n\t"
      "s_nop 0\n\t"
      "global_load_lds_dwordx4 v172, s[48:49]\n\t"
      "v_exp_f32 v80, v80\n\t"
      "v_exp_f32 v81, v81\n\t"
      "s_waitcnt lgkmcnt(2)\n\t"
      "v_smfmac_f32_32x32x32_bf16 v[0:15], v[144:147], v[72:79], %[idx]\n\t"
      "v_exp_f32 v82, v82\n\t"
      "v_exp_f32 v83, v83\n\t"
      "v_exp_f32 v84, v84\n\t"
      "v_mfma_f32_32x32x16_bf16 v[64:79], v[136:139], v[128:131], v[32:47]\n\t"
      "v_exp_f32 v85, v85\n\t"
      "v_exp_f32 v86, v86\n\t"
      "v_exp_f32 v87, v87\n\t"
      "v_exp_f32 v88, v88\n\t"
      "v_exp_f32 v89, v89\n\t"
      "v_smfmac_f32_32x32x32_bf16 v[16:31], v[140:143], v[80:87], %[idx]\n\t"
      "ds_read_b128 v[140:143], v177 offset:1024\n\t"
      "v_exp_f32 v90, v90\n\t"
      "v_exp_f32 v91, v91\n\t"
      "v_exp_f32 v92, v92\n\t"
      "v_mfma_f32_32x32x16_bf16 v[112:127], v[168:171], v[148:151], v[112:127]\n\t"
      "ds_read_b64_tr_b16 v[168:169], v179 offset:2048\n\t"
      "ds_read_b64_tr_b16 v[170:171], v179 offset:3072\n\t"
      "s_add_u32 %[t2], %[gr], 3\n\t"
      "s_cmp_ge_u32 %[t2], 7\n\t"
      "s_cselect_b32 vcc_lo, 7, 0\n\t"
      "s_sub_u32 %[t2], %[t2], vcc_lo\n\t"
      "s_lshl_b32 %[t2], %[t2], 12\n\t"
      "s_add_u32 %[t2], %[t2], %[ho]\n\t"
      "buffer_load_dwordx4 v[148:151], %[l16], %[rs], %[t2] offen offset:0 sc1\n\t"
      "v_exp_f32 v93, v93\n\t"
      "v_exp_f32 v94, v94\n\t"
      "v_exp_f32 v95, v95\n\t"
      "v_exp_f32 v64, v64\n\t"
      "v_exp_f32 v65, v65\n\t"
      "v_smfmac_f32_32x32x32_bf16 v[16:31], v[144:147], v[88:95], %[idx]\n\t"
      "ds_read_b128 v[144:147], v177 offset:1536\n\t"
      "v_exp_f32 v66, v66\n\t"
      "v_exp_f32 v67, v67\n\t"
      "v_exp_f32 v68, v68\n\t"
      "v_mfma_f32_32x32x16_bf16 v[80:95], v[136:139], v[132:135], v[48:63]\n\t"
      "ds_read_b128 v[136:139], v176 offset:2048\n\t"
      "v_exp_f32 v69, v69\n\t"
      "v_exp_f32 v70, v70\n\t"
      "v_exp_f32 v71, v71\n\t"
      "v_exp_f32 v72, v72\n\t"
      "v_exp_f32 v73, v73\n\t"
      "s_waitcnt lgkmcnt(4)\n\t"
      "v_smfmac_f32_32x32x32_bf16 v[0:15], v[140:143], v[64:71], %[idx]\n\t"
      "v_exp_f32 v74, v74\n\t"
      "v_exp_f32 v75, v75\n\t"
      "v_exp_f32 v76, v76\n\t"
      "v_mfma_f32_32x32x16_bf16 v[96:111], v[164:167], v[152:155], v[96:111]\n\t"
      "ds_read_b64_tr_b16 v[164:165], v178 offset:4096\n\t"
      "ds_read_b64_tr_b16 v[166:167], v178 offset:5120\n\t"
      "v_exp_f32 v77, v77\n\t"
      "v_exp_f32 v78, v78\n\t"
      "v_exp_f32 v79, v79\n\t"
      "s_setprio 1\n\t"
      "s_add_u32 m0, %[l4], 66560\n\t"
      "s_nop 0\n\t"
      "global_load_lds_dwordx4 v173, s[48:49]\n\t"
      "v_exp_f32 v80, v80\n\t"
      "v_exp_f32 v81, v81\n\t"
      "s_waitcnt lgkmcnt(2)\n\t"
      "v_smfmac_f32_32x32x32_bf16 v[0:15], v[144:147], v[72:79], %[idx]\n\t"
      "v_exp_f32 v82, v82\n\t"
      "v_exp_f32 v83, v83\n\t"
      "v_exp_f32 v84, v84\n\t"
      "v_mfma_f32_32x32x16_bf16 v[64:79], v[136:139], v[128:131], v[32:47]\n\t"
      "v_exp_f32 v85, v85\n\t"
      "v_exp_f32 v86, v86\n\t"
      "v_exp_f32 v87, v87\n\t"
      "v_exp_f32 v88, v88\n\t"
      "v_exp_f32 v89, v89\n\t"
      "v_smfmac_f32_32x32x32_bf16 v[16:31], v[140:143], v[80:87], %[idx]\n\t"
      "ds_read_b128 v[140:143], v177 offset:2048\n\t"
      "v_exp_f32 v90, v90\n\t"
      "v_exp_f32 v91, v91\n\t"
      "v_exp_f32 v92, v92\n\t"
      "v_mfma_f32_32x32x16_bf16 v[112:127], v[168:171], v[152:155], v[112:127]\n\t"
      "ds_read_b64_tr_b16 v[168:169], v179 offset:4096\n\t"
      "ds_read_b64_tr_b16 v[170:171], v179 offset:5120\n\t"
      "buffer_load_dwordx4 v[152:155], %[l16], %[rs], %[t2] offen offset:1024 sc1\n\t"
      "v_exp_f32 v93, v93\n\t"
      "v_exp_f32 v94, v94\n\t"
      "v_exp_f32 v95, v95\n\t"
      "s_add_u32 m0, %[l4], 67584\n\t"
      "s_nop 0\n\t"
      "global_load_lds_dwordx4 v174, s[48:49]\n\t"
      "v_exp_f32 v64, v64\n\t"
      "v_exp_f32 v65, v65\n\t"
      "v_smfmac_f32_32x32x32_bf16 v[16:31], v[144:147], v[88:95], %[idx]\n\t"
      "ds_read_b128 v[144:147], v177 offset:2560\n\t"
      "v_exp_f32 v66, v66\n\t"
      "v_exp_f32 v67, v67\n\t"
      "v_exp_f32 v68, v68\n\t"
      "v_mfma_f32_32x32x16_bf16 v[80:95], v[136:139], v[132:135], v[48:63]\n\t"
      "ds_read_b128 v[136:139], v176 offset:3072\n\t"
      "v_exp_f32 v69, v69\n\t"
      "v_exp_f32 v70, v70\n\t"
      "v_exp_f32 v71, v71\n\t"
      "v_exp_f32 v72, v72\n\t"
      "v_exp_f32 v73, v73\n\t"
      "s_waitcnt lgkmcnt(4)\n\t"
      "v_smfmac_f32_32x32x32_bf16 v[0:15], v[140:143], v[64:71], %[idx]\n\t"
      "v_exp_f32 v74, v74\n\t"
      "v_exp_f32 v75, v75\n\t"
      "v_exp_f32 v76, v76\n\t"
      "s_waitcnt vmcnt(6)\n\t"
      "v_mfma_f32_32x32x16_bf16 v[96:111], v[164:167], v[156:159], v[96:111]\n\t"
      "ds_read_b64_tr_b16 v[164:165], v178 offset:6144\n\t"
      "ds_read_b64_tr_b16 v[166:167], v178 offset:7168\n\t"
      "v_exp_f32 v77, v77\n\t"
      "v_exp_f32 v78, v78\n\t"
      "v_exp_f32 v79, v79\n\t"
      "s_setprio 0\n\t"
      "s_add_u32 m0, %[l4], 68608\n\t"
      "s_nop 0\n\t"
      "global_load_lds_dwordx4 v175, s[48:49]\n\t"
      "v_exp_f32 v80, v80\n\t"
      "v_exp_f32 v81, v81\n\t"
      "s_waitcnt lgkmcnt(2)\n\t"
      "v_smfmac_f32_32x32x32_bf16 v[0:15], v[144:147], v[72:79], %[idx]\n\t"
      "v_exp_f32 v82, v82\n\t"
      "v_exp_f32 v83, v83\n\t"
      "v_exp_f32 v84, v84\n\t"
      "v_mfma_f32_32x32x16_bf16 v[64:79], v[136:139], v[128:131], v[32:47]\n\t"
      "v_exp_f32 v85, v85\n\t"
      "v_exp_f32 v86, v86\n\t"
      "v_exp_f32 v87, v87\n\t"
      "v_exp_f32 v88, v88\n\t"
      "v_exp_f32 v89, v89\n\t"
      "v_smfmac_f32_32x32x32_bf16 v[16:31], v[140:143], v[80:87], %[idx]\n\t"
      "ds_read_b128 v[140:143], v177 offset:3072\n\t"
      "v_exp_f32 v90, v90\n\t"
      "v_exp_f32 v91, v91\n\t"
      "v_exp_f32 v92, v92\n\t"
      "v_mfma_f32_32x32x16_bf16 v[112:127], v[168:171], v[156:159], v[112:127]\n\t"
      "ds_read_b64_tr_b16 v[168:169], v179 offset:6144\n\t"
      "ds_read_b64_tr_b16 v[170:171], v179 offset:7168\n\t"
      "buffer_load_dwordx4 v[156:159], %[l16], %[rs], %[t2] offen offset:2048 sc1\n\t"
      "v_exp_f32 v93, v93\n\t"
      "v_exp_f32 v94, v94\n\t"
      "v_exp_f32 v95, v95\n\t"
      "v_exp_f32 v64, v64\n\t"
      "v_exp_f32 v65, v65\n\t"
      "v_smfmac_f32_32x32x32_bf16 v[16:31], v[144:147], v[88:95], %[idx]\n\t"
      "ds_read_b128 v[144:147], v177 offset:3584\n\t"
      "v_exp_f32 v66, v66\n\t"
      "v_exp_f32 v67, v67\n\t"
      "v_exp_f32 v68, v68\n\t"
      "v_mfma_f32_32x32x16_bf16 v[80:95], v[136:139], v[132:135], v[48:63]\n\t"
      "s_waitcnt vmcnt(7) lgkmcnt(1)\n\t"
      "s_barrier\n\t"
      "s_setprio 3\n\t"
      "v_add_u32 v178, 4294868992, v178\n\t"
      "v_add_u32 v179, 4294868992, v179\n\t"
      "v_add_u32 v176, 4294868992, v176\n\t"
      "ds_read_b128 v[136:139], v176 offset:0\n\t"
      "v_exp_f32 v69, v69\n\t"
      "v_exp_f32 v70, v70\n\t"
      "v_exp_f32 v71, v71\n\t"
      "v_exp_f32 v72, v72\n\t"
      "v_exp_f32 v73, v73\n\t"
      "v_smfmac_f32_32x32x32_bf16 v[0:15], v[140:143], v[64:71], %[idx]\n\t"
      "v_exp_f32 v74, v74\n\t"
      "v_exp_f32 v75, v75\n\t"
      "v_exp_f32 v76, v76\n\t"
      "v_mfma_f32_32x32x16_bf16 v[96:111], v[164:167], v[160:163], v[96:111]\n\t"
      "ds_read_b64_tr_b16 v[164:165], v178 offset:0\n\t"
      "ds_read_b64_tr_b16 v[166:167], v178 offset:1024\n\t"
      "v_exp_f32 v77, v77\n\t"
      "v_exp_f32 v78, v78\n\t"
      "v_exp_f32 v79, v79\n\t"
      "s_add_u32 m0, %[l1], 49152\n\t"
      "s_add_u32 %[t1], %[ko], 57344\n\t"
      "buffer_load_dwordx4 %[l16], %[rs], %[t1] offen sc1 lds\n\t"
      "v_exp_f32 v80, v80\n\t"
      "v_exp_f32 v81, v81\n\t"
      "s_waitcnt lgkmcnt(2)\n\t"
      "v_smfmac_f32_32x32x32_bf16 v[0:15], v[144:147], v[72:79], %[idx]\n\t"
      "v_exp_f32 v82, v82\n\t"
      "v_exp_f32 v83, v83\n\t"
      "v_exp_f32 v84, v84\n\t"
      "v_mfma_f32_32x32x16_bf16 v[64:79], v[136:139], v[128:131], v[32:47]\n\t"
      "v_exp_f32 v85, v85\n\t"
      "v_exp_f32 v86, v86\n\t"
      "v_exp_f32 v87, v87\n\t"
      "v_exp_f32 v88, v88\n\t"
      "v_exp_f32 v89, v89\n\t"
      "v_smfmac_f32_32x32x32_bf16 v[16:31], v[140:143], v[80:87], %[idx]\n\t"
      "s_mov_b32 %[t1], 4294868992\n\t"
      "v_mad_i32_i24 v177, %[t1], v180, v177\n\t"
      "ds_read_b128 v[140:143], v177 offset:0\n\t"
      "v_exp_f32 v90, v90\n\t"
      "v_exp_f32 v91, v91\n\t"
      "v_exp_f32 v92, v92\n\t"
      "v_mfma_f32_32x32x16_bf16 v[112:127], v[168:171], v[160:163], v[112:127]\n\t"
      "ds_read_b64_tr_b16 v[168:169], v179 offset:0\n\t"
      "ds_read_b64_tr_b16 v[170:171], v179 offset:1024\n\t"
      "buffer_load_dwordx4 v[160:163], %[l16], %[rs], %[t2] offen offset:3072 sc1\n\t"
      "v_exp_f32 v93, v93\n\t"
      "v_exp_f32 v94, v94\n\t"
      "v_exp_f32 v95, v95\n\t"
      "s_add_u32 m0, %[l1], 57344\n\t"
      "s_add_u32 %[t1], %[ko], 2154496\n\t"
      "buffer_load_dwordx4 %[l16], %[rs], %[t1] offen sc1 lds\n\t"
      "v_exp_f32 v64, v64\n\t"
      "v_exp_f32 v65, v65\n\t"
      "v_smfmac_f32_32x32x32_bf16 v[16:31], v[144:147], v[88:95], %[idx]\n\t"
      "ds_read_b128 v[144:147], v177 offset:512\n\t"
      "v_exp_f32 v66, v66\n\t"
      "v_exp_f32 v67, v67\n\t"
      "v_exp_f32 v68, v68\n\t"
      "v_mfma_f32_32x32x16_bf16 v[80:95], v[136:139], v[132:135], v[48:63]\n\t"
      "ds_read_b128 v[136:139], v176 offset:1024\n\t"
      "v_exp_f32 v69, v69\n\t"
      "v_exp_f32 v70, v70\n\t"
      "v_exp_f32 v71, v71\n\t"
      "v_exp_f32 v72, v72\n\t"
      "v_exp_f32 v73, v73\n\t"
      "s_waitcnt lgkmcnt(4)\n\t"
      "v_smfmac_f32_32x32x32_bf16 v[0:15], v[140:143], v[64:71], %[idx]\n\t"
      "v_exp_f32 v74, v74\n\t"
      "v_exp_f32 v75, v75\n\t"
      "v_exp_f32 v76, v76\n\t"
      "s_waitcnt vmcnt(6)\n\t"
      "v_mfma_f32_32x32x16_bf16 v[96:111], v[164:167], v[148:151], v[96:111]\n\t"
      "ds_read_b64_tr_b16 v[164:165], v178 offset:2048\n\t"
      "ds_read_b64_tr_b16 v[166:167], v178 offset:3072\n\t"
      "s_add_u32 %[t3], %[gr], 5\n\t"
      "s_cmp_ge_u32 %[t3], 7\n\t"
      "s_cselect_b32 vcc_lo, 7, 0\n\t"
      "s_sub_u32 %[t3], %[t3], vcc_lo\n\t"
      "s_lshl_b32 %[t3], %[t3], 12\n\t"
      "s_add_u32 %[t3], %[t3], %[ho]\n\t"
      "buffer_load_dwordx4 v[212:215], %[l16], %[rs], %[t3] offen offset:0 sc1\n\t"
      "v_exp_f32 v77, v77\n\t"
      "v_exp_f32 v78, v78\n\t"
      "v_exp_f32 v79, v79\n\t"
      "s_setprio 2\n\t"
      "s_add_u32 %[t1], %[gr], 5\n\t"
      "s_cmp_ge_u32 %[t1], 7\n\t"
      "s_cselect_b32 vcc_lo, 7, 0\n\t"
      "s_sub_u32 %[t1], %[t1], vcc_lo\n\t"
      "s_lshl_b32 %[t1], %[t1], 16\n\t"
      "s_add_u32 s48, s44, %[t1]\n\t"
      "s_addc_u32 s49, s45, 0\n\t"
      "s_add_u32 m0, %[l4], 114688\n\t"
      "s_nop 0\n\t"
      "global_load_lds_dwordx4 v172, s[48:49]\n\t"
      "v_exp_f32 v80, v80\n\t"
      "v_exp_f32 v81, v81\n\t"
      "s_waitcnt lgkmcnt(2)\n\t"
      "v_smfmac_f32_32x32x32_bf16 v[0:15], v[144:147], v[72:79], %[idx]\n\t"
      "v_exp_f32 v82, v82\n\t"
      "v_exp_f32 v83, v83\n\t"
      "v_exp_f32 v84, v84\n\t"
      "v_mfma_f32_32x32x16_bf16 v[64:79], v[136:139], v[128:131], v[32:47]\n\t"
      "v_exp_f32 v85, v85\n\t"
      "v_exp_f32 v86, v86\n\t"
      "v_exp_f32 v87, v87\n\t"
      "v_exp_f32 v88, v88\n\t"
      "v_exp_f32 v89, v89\n\t"
      "v_smfmac_f32_32x32x32_bf16 v[16:31], v[140:143], v[80:87], %[idx]\n\t"
      "ds_read_b128 v[140:143], v177 offset:1024\n\t"
      "v_exp_f32 v90, v90\n\t"
      "v_exp_f32 v91, v91\n\t"
      "v_exp_f32 v92, v92\n\t"
      "v_mfma_f32_32x32x16_bf16 v[112:127], v[168:171], v[148:151], v[112:127]\n\t"
      "ds_read_b64_tr_b16 v[168:169], v179 offset:2048\n\t"
      "ds_read_b64_tr_b16 v[170:171], v179 offset:3072\n\t"
      "s_add_u32 %[t2], %[gr], 4\n\t"
      "s_cmp_ge_u32 %[t2], 7\n\t"
      "s_cselect_b32 vcc_lo, 7, 0\n\t"
      "s_sub_u32 %[t2], %[t2], vcc_lo\n\t"
      "s_lshl_b32 %[t2], %[t2], 12\n\t"
      "s_add_u32 %[t2], %[t2], %[ho]\n\t"
      "buffer_load_dwordx4 v[148:151], %[l16], %[rs], %[t2] offen offset:0 sc1\n\t"
      "v_exp_f32 v93, v93\n\t"
      "v_exp_f32 v94, v94\n\t"
      "v_exp_f32 v95, v95\n\t"
      "v_exp_f32 v64, v64\n\t"
      "v_exp_f32 v65, v65\n\t"
      "v_smfmac_f32_32x32x32_bf16 v[16:31], v[144:147], v[88:95], %[idx]\n\t"
      "ds_read_b128 v[144:147], v177 offset:1536\n\t"
      "v_exp_f32 v66, v66\n\t"
      "v_exp_f32 v67, v67\n\t"
      "v_exp_f32 v68, v68\n\t"
      "v_mfma_f32_32x32x16_bf16 v[80:95], v[136:139], v[132:135], v[48:63]\n\t"
      "ds_read_b128 v[136:139], v176 offset:2048\n\t"
      "v_exp_f32 v69, v69\n\t"
      "v_exp_f32 v70, v70\n\t"
      "v_exp_f32 v71, v71\n\t"
      "v_exp_f32 v72, v72\n\t"
      "v_exp_f32 v73, v73\n\t"
      "s_waitcnt lgkmcnt(4)\n\t"
      "v_smfmac_f32_32x32x32_bf16 v[0:15], v[140:143], v[64:71], %[idx]\n\t"
      "v_exp_f32 v74, v74\n\t"
      "v_exp_f32 v75, v75\n\t"
      "v_exp_f32 v76, v76\n\t"
      "v_mfma_f32_32x32x16_bf16 v[96:111], v[164:167], v[152:155], v[96:111]\n\t"
      "ds_read_b64_tr_b16 v[164:165], v178 offset:4096\n\t"
      "ds_read_b64_tr_b16 v[166:167], v178 offset:5120\n\t"
      "buffer_load_dwordx4 v[216:219], %[l16], %[rs], %[t3] offen offset:1024 sc1\n\t"
      "v_exp_f32 v77, v77\n\t"
      "v_exp_f32 v78, v78\n\t"
      "v_exp_f32 v79, v79\n\t"
      "s_setprio 1\n\t"
      "s_add_u32 m0, %[l4], 115712\n\t"
      "s_nop 0\n\t"
      "global_load_lds_dwordx4 v173, s[48:49]\n\t"
      "v_exp_f32 v80, v80\n\t"
      "v_exp_f32 v81, v81\n\t"
      "s_waitcnt lgkmcnt(2)\n\t"
      "v_smfmac_f32_32x32x32_bf16 v[0:15], v[144:147], v[72:79], %[idx]\n\t"
      "v_exp_f32 v82, v82\n\t"
      "v_exp_f32 v83, v83\n\t"
      "v_exp_f32 v84, v84\n\t"
      "v_mfma_f32_32x32x16_bf16 v[64:79], v[136:139], v[128:131], v[32:47]\n\t"
      "v_exp_f32 v85, v85\n\t"
      "v_exp_f32 v86, v86\n\t"
      "v_exp_f32 v87, v87\n\t"
      "v_exp_f32 v88, v88\n\t"
      "v_exp_f32 v89, v89\n\t"
      "v_smfmac_f32_32x32x32_bf16 v[16:31], v[140:143], v[80:87], %[idx]\n\t"
      "ds_read_b128 v[140:143], v177 offset:2048\n\t"
      "v_exp_f32 v90, v90\n\t"
      "v_exp_f32 v91, v91\n\t"
      "v_exp_f32 v92, v92\n\t"
      "v_mfma_f32_32x32x16_bf16 v[112:127], v[168:171], v[152:155], v[112:127]\n\t"
      "ds_read_b64_tr_b16 v[168:169], v179 offset:4096\n\t"
      "ds_read_b64_tr_b16 v[170:171], v179 offset:5120\n\t"
      "buffer_load_dwordx4 v[152:155], %[l16], %[rs], %[t2] offen offset:1024 sc1\n\t"
      "v_exp_f32 v93, v93\n\t"
      "v_exp_f32 v94, v94\n\t"
      "v_exp_f32 v95, v95\n\t"
      "s_add_u32 m0, %[l4], 116736\n\t"
      "s_nop 0\n\t"
      "global_load_lds_dwordx4 v174, s[48:49]\n\t"
      "v_exp_f32 v64, v64\n\t"
      "v_exp_f32 v65, v65\n\t"
      "v_smfmac_f32_32x32x32_bf16 v[16:31], v[144:147], v[88:95], %[idx]\n\t"
      "ds_read_b128 v[144:147], v177 offset:2560\n\t"
      "v_exp_f32 v66, v66\n\t"
      "v_exp_f32 v67, v67\n\t"
      "v_exp_f32 v68, v68\n\t"
      "v_mfma_f32_32x32x16_bf16 v[80:95], v[136:139], v[132:135], v[48:63]\n\t"
      "ds_read_b128 v[136:139], v176 offset:3072\n\t"
      "v_exp_f32 v69, v69\n\t"
      "v_exp_f32 v70, v70\n\t"
      "v_exp_f32 v71, v71\n\t"
      "v_exp_f32 v72, v72\n\t"
      "v_exp_f32 v73, v73\n\t"
      "s_waitcnt lgkmcnt(4)\n\t"
      "v_smfmac_f32_32x32x32_bf16 v[0:15], v[140:143], v[64:71], %[idx]\n\t"
      "v_exp_f32 v74, v74\n\t"
      "v_exp_f32 v75, v75\n\t"
      "v_exp_f32 v76, v76\n\t"
      "s_waitcnt vmcnt(8)\n\t"
      "v_mfma_f32_32x32x16_bf16 v[96:111], v[164:167], v[156:159], v[96:111]\n\t"
      "ds_read_b64_tr_b16 v[164:165], v178 offset:6144\n\t"
      "ds_read_b64_tr_b16 v[166:167], v178 offset:7168\n\t"
      "buffer_load_dwordx4 v[220:223], %[l16], %[rs], %[t3] offen offset:2048 sc1\n\t"
      "v_exp_f32 v77, v77\n\t"
      "v_exp_f32 v78, v78\n\t"
      "v_exp_f32 v79, v79\n\t"
      "s_setprio 0\n\t"
      "s_add_u32 m0, %[l4], 117760\n\t"
      "s_nop 0\n\t"
      "global_load_lds_dwordx4 v175, s[48:49]\n\t"
      "v_exp_f32 v80, v80\n\t"
      "v_exp_f32 v81, v81\n\t"
      "s_waitcnt lgkmcnt(2)\n\t"
      "v_smfmac_f32_32x32x32_bf16 v[0:15], v[144:147], v[72:79], %[idx]\n\t"
      "v_exp_f32 v82, v82\n\t"
      "v_exp_f32 v83, v83\n\t"
      "v_exp_f32 v84, v84\n\t"
      "v_mfma_f32_32x32x16_bf16 v[64:79], v[136:139], v[128:131], v[32:47]\n\t"
      "v_exp_f32 v85, v85\n\t"
      "v_exp_f32 v86, v86\n\t"
      "v_exp_f32 v87, v87\n\t"
      "v_exp_f32 v88, v88\n\t"
      "v_exp_f32 v89, v89\n\t"
      "v_smfmac_f32_32x32x32_bf16 v[16:31], v[140:143], v[80:87], %[idx]\n\t"
      "ds_read_b128 v[140:143], v177 offset:3072\n\t"
      "v_exp_f32 v90, v90\n\t"
      "v_exp_f32 v91, v91\n\t"
      "v_exp_f32 v92, v92\n\t"
      "v_mfma_f32_32x32x16_bf16 v[112:127], v[168:171], v[156:159], v[112:127]\n\t"
      "ds_read_b64_tr_b16 v[168:169], v179 offset:6144\n\t"
      "ds_read_b64_tr_b16 v[170:171], v179 offset:7168\n\t"
      "buffer_load_dwordx4 v[156:159], %[l16], %[rs], %[t2] offen offset:2048 sc1\n\t"
      "v_exp_f32 v93, v93\n\t"
      "v_exp_f32 v94, v94\n\t"
      "v_exp_f32 v95, v95\n\t"
      "v_lshrrev_b32 v187, 2, %[l16]\n\t"
      "v_and_b32 v187, 0x7c, v187\n\t"
      "buffer_load_dword v208, v187, %[rs], %[ga] offen sc1\n\t"
      "v_exp_f32 v64, v64\n\t"
      "v_exp_f32 v65, v65\n\t"
      "v_smfmac_f32_32x32x32_bf16 v[16:31], v[144:147], v[88:95], %[idx]\n\t"
      "ds_read_b128 v[144:147], v177 offset:3584\n\t"
      "v_exp_f32 v66, v66\n\t"
      "v_exp_f32 v67, v67\n\t"
      "v_exp_f32 v68, v68\n\t"
      "v_mfma_f32_32x32x16_bf16 v[80:95], v[136:139], v[132:135], v[48:63]\n\t"
      "s_waitcnt vmcnt(11) lgkmcnt(1)\n\t"
      "s_barrier\n\t"
      "s_setprio 3\n\t"
      "v_add_u32 v178, 49152, v178\n\t"
      "v_add_u32 v179, 49152, v179\n\t"
      "v_add_u32 v176, 49152, v176\n\t"
      "ds_read_b128 v[136:139], v176 offset:0\n\t"
      "v_exp_f32 v69, v69\n\t"
      "v_exp_f32 v70, v70\n\t"
      "v_exp_f32 v71, v71\n\t"
      "v_exp_f32 v72, v72\n\t"
      "v_exp_f32 v73, v73\n\t"
      "v_smfmac_f32_32x32x32_bf16 v[0:15], v[140:143], v[64:71], %[idx]\n\t"
      "v_exp_f32 v74, v74\n\t"
      "v_exp_f32 v75, v75\n\t"
      "v_exp_f32 v76, v76\n\t"
      "v_mfma_f32_32x32x16_bf16 v[96:111], v[164:167], v[160:163], v[96:111]\n\t"
      "ds_read_b64_tr_b16 v[164:165], v178 offset:0\n\t"
      "ds_read_b64_tr_b16 v[166:167], v178 offset:1024\n\t"
      "buffer_load_dwordx4 v[224:227], %[l16], %[rs], %[t3] offen offset:3072 sc1\n\t"
      "v_exp_f32 v77, v77\n\t"
      "v_exp_f32 v78, v78\n\t"
      "v_exp_f32 v79, v79\n\t"
      "s_add_u32 m0, %[l2], 98304\n\t"
      "s_nop 0\n\t"
      "global_load_lds_dwordx4 v172, s[46:47]\n\t"
      "v_exp_f32 v80, v80\n\t"
      "v_exp_f32 v81, v81\n\t"
      "s_waitcnt lgkmcnt(2)\n\t"
      "v_smfmac_f32_32x32x32_bf16 v[0:15], v[144:147], v[72:79], %[idx]\n\t"
      "v_exp_f32 v82, v82\n\t"
      "v_exp_f32 v83, v83\n\t"
      "v_exp_f32 v84, v84\n\t"
      "v_mfma_f32_32x32x16_bf16 v[64:79], v[136:139], v[128:131], v[32:47]\n\t"
      "v_exp_f32 v85, v85\n\t"
      "v_exp_f32 v86, v86\n\t"
      "v_exp_f32 v87, v87\n\t"
      "v_exp_f32 v88, v88\n\t"
      "v_exp_f32 v89, v89\n\t"
      "v_smfmac_f32_32x32x32_bf16 v[16:31], v[140:143], v[80:87], %[idx]\n\t"
      "s_mov_b32 %[t1], 49152\n\t"
      "v_mad_i32_i24 v177, %[t1], v180, v177\n\t"
      "ds_read_b128 v[140:143], v177 offset:0\n\t"
      "v_exp_f32 v90, v90\n\t"
      "v_exp_f32 v91, v91\n\t"
      "v_exp_f32 v92, v92\n\t"
      "v_mfma_f32_32x32x16_bf16 v[112:127], v[168:171], v[160:163], v[112:127]\n\t"
      "ds_read_b64_tr_b16 v[168:169], v179 offset:0\n\t"
      "ds_read_b64_tr_b16 v[170:171], v179 offset:1024\n\t"
      "buffer_load_dwordx4 v[160:163], %[l16], %[rs], %[t2] offen offset:3072 sc1\n\t"
      "v_exp_f32 v93, v93\n\t"
      "v_exp_f32 v94, v94\n\t"
      "v_exp_f32 v95, v95\n\t"
      "s_add_u32 m0, %[l2], 99328\n\t"
      "s_nop 0\n\t"
      "global_load_lds_dwordx4 v173, s[46:47]\n\t"
      "s_add_u32 s46, s46, 0x8000\n\t"
      "s_addc_u32 s47, s47, 0\n\t"
      "v_exp_f32 v64, v64\n\t"
      "v_exp_f32 v65, v65\n\t"
      "v_smfmac_f32_32x32x32_bf16 v[16:31], v[144:147], v[88:95], %[idx]\n\t"
      "ds_read_b128 v[144:147], v177 offset:512\n\t"
      "v_exp_f32 v66, v66\n\t"
      "v_exp_f32 v67, v67\n\t"
      "v_exp_f32 v68, v68\n\t"
      "v_mfma_f32_32x32x16_bf16 v[80:95], v[136:139], v[132:135], v[48:63]\n\t"
      "ds_read_b128 v[136:139], v176 offset:1024\n\t"
      "v_exp_f32 v69, v69\n\t"
      "v_exp_f32 v70, v70\n\t"
      "v_exp_f32 v71, v71\n\t"
      "v_exp_f32 v72, v72\n\t"
      "v_exp_f32 v73, v73\n\t"
      "s_waitcnt lgkmcnt(4)\n\t"
      "v_smfmac_f32_32x32x32_bf16 v[0:15], v[140:143], v[64:71], %[idx]\n\t"
      "v_exp_f32 v74, v74\n\t"
      "v_exp_f32 v75, v75\n\t"
      "v_exp_f32 v76, v76\n\t"
      "s_waitcnt vmcnt(9)\n\t"
      "v_mfma_f32_32x32x16_bf16 v[96:111], v[164:167], v[148:151], v[96:111]\n\t"
      "ds_read_b64_tr_b16 v[164:165], v178 offset:2048\n\t"
      "ds_read_b64_tr_b16 v[166:167], v178 offset:3072\n\t"
      "s_add_u32 %[t3], %[gr], 6\n\t"
      "s_cmp_ge_u32 %[t3], 7\n\t"
      "s_cselect_b32 vcc_lo, 7, 0\n\t"
      "s_sub_u32 %[t3], %[t3], vcc_lo\n\t"
      "s_lshl_b32 %[t3], %[t3], 12\n\t"
      "s_add_u32 %[t3], %[t3], %[ho]\n\t"
      "buffer_load_dwordx4 v[192:195], %[l16], %[rs], %[t3] offen offset:0 sc1\n\t"
      "v_exp_f32 v77, v77\n\t"
      "v_exp_f32 v78, v78\n\t"
      "v_exp_f32 v79, v79\n\t"
      "s_setprio 2\n\t"
      "s_add_u32 %[t1], %[gr], 6\n\t"
      "s_cmp_ge_u32 %[t1], 7\n\t"
      "s_cselect_b32 vcc_lo, 7, 0\n\t"
      "s_sub_u32 %[t1], %[t1], vcc_lo\n\t"
      "s_lshl_b32 %[t1], %[t1], 16\n\t"
      "s_add_u32 s48, s44, %[t1]\n\t"
      "s_addc_u32 s49, s45, 0\n\t"
      "s_add_u32 m0, %[l4], 16384\n\t"
      "s_nop 0\n\t"
      "global_load_lds_dwordx4 v172, s[48:49]\n\t"
      "v_exp_f32 v80, v80\n\t"
      "v_exp_f32 v81, v81\n\t"
      "s_waitcnt lgkmcnt(2)\n\t"
      "v_smfmac_f32_32x32x32_bf16 v[0:15], v[144:147], v[72:79], %[idx]\n\t"
      "v_exp_f32 v82, v82\n\t"
      "v_exp_f32 v83, v83\n\t"
      "v_exp_f32 v84, v84\n\t"
      "v_mfma_f32_32x32x16_bf16 v[64:79], v[136:139], v[128:131], v[32:47]\n\t"
      "v_exp_f32 v85, v85\n\t"
      "v_exp_f32 v86, v86\n\t"
      "v_exp_f32 v87, v87\n\t"
      "v_exp_f32 v88, v88\n\t"
      "v_exp_f32 v89, v89\n\t"
      "v_smfmac_f32_32x32x32_bf16 v[16:31], v[140:143], v[80:87], %[idx]\n\t"
      "ds_read_b128 v[140:143], v177 offset:1024\n\t"
      "v_exp_f32 v90, v90\n\t"
      "v_exp_f32 v91, v91\n\t"
      "v_exp_f32 v92, v92\n\t"
      "v_mfma_f32_32x32x16_bf16 v[112:127], v[168:171], v[148:151], v[112:127]\n\t"
      "ds_read_b64_tr_b16 v[168:169], v179 offset:2048\n\t"
      "ds_read_b64_tr_b16 v[170:171], v179 offset:3072\n\t"
      "buffer_load_dwordx4 v[196:199], %[l16], %[rs], %[t3] offen offset:1024 sc1\n\t"
      "v_exp_f32 v93, v93\n\t"
      "v_exp_f32 v94, v94\n\t"
      "v_exp_f32 v95, v95\n\t"
      "v_exp_f32 v64, v64\n\t"
      "v_exp_f32 v65, v65\n\t"
      "v_smfmac_f32_32x32x32_bf16 v[16:31], v[144:147], v[88:95], %[idx]\n\t"
      "ds_read_b128 v[144:147], v177 offset:1536\n\t"
      "v_exp_f32 v66, v66\n\t"
      "v_exp_f32 v67, v67\n\t"
      "v_exp_f32 v68, v68\n\t"
      "v_mfma_f32_32x32x16_bf16 v[80:95], v[136:139], v[132:135], v[48:63]\n\t"
      "ds_read_b128 v[136:139], v176 offset:2048\n\t"
      "v_exp_f32 v69, v69\n\t"
      "v_exp_f32 v70, v70\n\t"
      "v_exp_f32 v71, v71\n\t"
      "v_exp_f32 v72, v72\n\t"
      "v_exp_f32 v73, v73\n\t"
      "s_waitcnt lgkmcnt(4)\n\t"
      "v_smfmac_f32_32x32x32_bf16 v[0:15], v[140:143], v[64:71], %[idx]\n\t"
      "v_exp_f32 v74, v74\n\t"
      "v_exp_f32 v75, v75\n\t"
      "v_exp_f32 v76, v76\n\t"
      "v_mfma_f32_32x32x16_bf16 v[96:111], v[164:167], v[152:155], v[96:111]\n\t"
      "ds_read_b64_tr_b16 v[164:165], v178 offset:4096\n\t"
      "ds_read_b64_tr_b16 v[166:167], v178 offset:5120\n\t"
      "buffer_load_dwordx4 v[200:203], %[l16], %[rs], %[t3] offen offset:2048 sc1\n\t"
      "v_exp_f32 v77, v77\n\t"
      "v_exp_f32 v78, v78\n\t"
      "v_exp_f32 v79, v79\n\t"
      "s_setprio 1\n\t"
      "s_add_u32 m0, %[l4], 17408\n\t"
      "s_nop 0\n\t"
      "global_load_lds_dwordx4 v173, s[48:49]\n\t"
      "v_exp_f32 v80, v80\n\t"
      "v_exp_f32 v81, v81\n\t"
      "s_waitcnt lgkmcnt(2)\n\t"
      "v_smfmac_f32_32x32x32_bf16 v[0:15], v[144:147], v[72:79], %[idx]\n\t"
      "v_exp_f32 v82, v82\n\t"
      "v_exp_f32 v83, v83\n\t"
      "v_exp_f32 v84, v84\n\t"
      "v_mfma_f32_32x32x16_bf16 v[64:79], v[136:139], v[128:131], v[32:47]\n\t"
      "v_exp_f32 v85, v85\n\t"
      "v_exp_f32 v86, v86\n\t"
      "v_exp_f32 v87, v87\n\t"
      "v_exp_f32 v88, v88\n\t"
      "v_exp_f32 v89, v89\n\t"
      "v_smfmac_f32_32x32x32_bf16 v[16:31], v[140:143], v[80:87], %[idx]\n\t"
      "ds_read_b128 v[140:143], v177 offset:2048\n\t"
      "v_exp_f32 v90, v90\n\t"
      "v_exp_f32 v91, v91\n\t"
      "v_exp_f32 v92, v92\n\t"
      "v_mfma_f32_32x32x16_bf16 v[112:127], v[168:171], v[152:155], v[112:127]\n\t"
      "ds_read_b64_tr_b16 v[168:169], v179 offset:4096\n\t"
      "ds_read_b64_tr_b16 v[170:171], v179 offset:5120\n\t"
      "buffer_load_dwordx4 v[204:207], %[l16], %[rs], %[t3] offen offset:3072 sc1\n\t"
      "v_exp_f32 v93, v93\n\t"
      "v_exp_f32 v94, v94\n\t"
      "v_exp_f32 v95, v95\n\t"
      "s_add_u32 m0, %[l4], 18432\n\t"
      "s_nop 0\n\t"
      "global_load_lds_dwordx4 v174, s[48:49]\n\t"
      "v_exp_f32 v64, v64\n\t"
      "v_exp_f32 v65, v65\n\t"
      "v_smfmac_f32_32x32x32_bf16 v[16:31], v[144:147], v[88:95], %[idx]\n\t"
      "ds_read_b128 v[144:147], v177 offset:2560\n\t"
      "v_exp_f32 v66, v66\n\t"
      "v_exp_f32 v67, v67\n\t"
      "v_exp_f32 v68, v68\n\t"
      "v_mfma_f32_32x32x16_bf16 v[80:95], v[136:139], v[132:135], v[48:63]\n\t"
      "ds_read_b128 v[136:139], v176 offset:3072\n\t"
      "v_exp_f32 v69, v69\n\t"
      "v_exp_f32 v70, v70\n\t"
      "v_exp_f32 v71, v71\n\t"
      "v_exp_f32 v72, v72\n\t"
      "v_exp_f32 v73, v73\n\t"
      "s_waitcnt lgkmcnt(4)\n\t"
      "v_smfmac_f32_32x32x32_bf16 v[0:15], v[140:143], v[64:71], %[idx]\n\t"
      "v_exp_f32 v74, v74\n\t"
      "v_exp_f32 v75, v75\n\t"
      "v_exp_f32 v76, v76\n\t"
      "s_waitcnt vmcnt(8)\n\t"
      "v_mfma_f32_32x32x16_bf16 v[96:111], v[164:167], v[156:159], v[96:111]\n\t"
      "ds_read_b64_tr_b16 v[164:165], v178 offset:6144\n\t"
      "ds_read_b64_tr_b16 v[166:167], v178 offset:7168\n\t"
      "v_exp_f32 v77, v77\n\t"
      "v_exp_f32 v78, v78\n\t"
      "v_exp_f32 v79, v79\n\t"
      "s_setprio 0\n\t"
      "s_add_u32 m0, %[l4], 19456\n\t"
      "s_nop 0\n\t"
      "global_load_lds_dwordx4 v175, s[48:49]\n\t"
      "v_exp_f32 v80, v80\n\t"
      "v_exp_f32 v81, v81\n\t"
      "s_waitcnt lgkmcnt(2)\n\t"
      "v_smfmac_f32_32x32x32_bf16 v[0:15], v[144:147], v[72:79], %[idx]\n\t"
      "v_exp_f32 v82, v82\n\t"
      "v_exp_f32 v83, v83\n\t"
      "v_exp_f32 v84, v84\n\t"
      "v_mfma_f32_32x32x16_bf16 v[64:79], v[136:139], v[128:131], v[32:47]\n\t"
      "v_exp_f32 v85, v85\n\t"
      "v_exp_f32 v86, v86\n\t"
      "v_exp_f32 v87, v87\n\t"
      "v_exp_f32 v88, v88\n\t"
      "v_exp_f32 v89, v89\n\t"
      "v_smfmac_f32_32x32x32_bf16 v[16:31], v[140:143], v[80:87], %[idx]\n\t"
      "ds_read_b128 v[140:143], v177 offset:3072\n\t"
      "v_exp_f32 v90, v90\n\t"
      "v_exp_f32 v91, v91\n\t"
      "v_exp_f32 v92, v92\n\t"
      "v_mfma_f32_32x32x16_bf16 v[112:127], v[168:171], v[156:159], v[112:127]\n\t"
      "ds_read_b64_tr_b16 v[168:169], v179 offset:6144\n\t"
      "ds_read_b64_tr_b16 v[170:171], v179 offset:7168\n\t"
      "v_exp_f32 v93, v93\n\t"
      "v_exp_f32 v94, v94\n\t"
      "v_exp_f32 v95, v95\n\t"
      "v_exp_f32 v64, v64\n\t"
      "v_exp_f32 v65, v65\n\t"
      "v_smfmac_f32_32x32x32_bf16 v[16:31], v[144:147], v[88:95], %[idx]\n\t"
      "ds_read_b128 v[144:147], v177 offset:3584\n\t"
      "v_exp_f32 v66, v66\n\t"
      "v_exp_f32 v67, v67\n\t"
      "v_exp_f32 v68, v68\n\t"
      "v_mfma_f32_32x32x16_bf16 v[80:95], v[136:139], v[132:135], v[48:63]\n\t"
      "s_waitcnt vmcnt(14) lgkmcnt(1)\n\t"
      "s_barrier\n\t"
      "s_setprio 3\n\t"
      "v_add_u32 v178, 49152, v178\n\t"
      "v_add_u32 v179, 49152, v179\n\t"
      "v_exp_f32 v69, v69\n\t"
      "v_exp_f32 v70, v70\n\t"
      "v_exp_f32 v71, v71\n\t"
      "v_exp_f32 v72, v72\n\t"
      "v_exp_f32 v73, v73\n\t"
      "v_smfmac_f32_32x32x32_bf16 v[0:15], v[140:143], v[64:71], %[idx]\n\t"
      "v_exp_f32 v74, v74\n\t"
      "v_exp_f32 v75, v75\n\t"
      "v_exp_f32 v76, v76\n\t"
      "v_mfma_f32_32x32x16_bf16 v[96:111], v[164:167], v[160:163], v[96:111]\n\t"
      "ds_read_b64_tr_b16 v[164:165], v178 offset:0\n\t"
      "ds_read_b64_tr_b16 v[166:167], v178 offset:1024\n\t"
      "v_exp_f32 v77, v77\n\t"
      "v_exp_f32 v78, v78\n\t"
      "v_exp_f32 v79, v79\n\t"
      "s_add_u32 m0, %[l2], 65536\n\t"
      "s_nop 0\n\t"
      "global_load_lds_dwordx4 v172, s[46:47]\n\t"
      "s_add_u32 m0, %[l2], 66560\n\t"
      "s_nop 0\n\t"
      "global_load_lds_dwordx4 v173, s[46:47]\n\t"
      "s_add_u32 s46, s46, 0x8000\n\t"
      "s_addc_u32 s47, s47, 0\n\t"
      "v_exp_f32 v80, v80\n\t"
      "v_exp_f32 v81, v81\n\t"
      "s_waitcnt lgkmcnt(2)\n\t"
      "v_smfmac_f32_32x32x32_bf16 v[0:15], v[144:147], v[72:79], %[idx]\n\t"
      "v_exp_f32 v82, v82\n\t"
      "v_exp_f32 v83, v83\n\t"
      "v_exp_f32 v84, v84\n\t"
      "v_exp_f32 v85, v85\n\t"
      "v_exp_f32 v86, v86\n\t"
      "v_exp_f32 v87, v87\n\t"
      "v_exp_f32 v88, v88\n\t"
      "v_exp_f32 v89, v89\n\t"
      "v_smfmac_f32_32x32x32_bf16 v[16:31], v[140:143], v[80:87], %[idx]\n\t"
      "v_exp_f32 v90, v90\n\t"
      "v_exp_f32 v91, v91\n\t"
      "v_exp_f32 v92, v92\n\t"
      "v_mfma_f32_32x32x16_bf16 v[112:127], v[168:171], v[160:163], v[112:127]\n\t"
      "ds_read_b64_tr_b16 v[168:169], v179 offset:0\n\t"
      "ds_read_b64_tr_b16 v[170:171], v179 offset:1024\n\t"
      "v_exp_f32 v93, v93\n\t"
      "v_exp_f32 v94, v94\n\t"
      "v_exp_f32 v95, v95\n\t"
      "v_add_u32 v176, %[l2], %[l16]\n\t"
      "s_lshr_b32 %[t1], %[l1], 2\n\t"
      "s_add_u32 %[t1], %[t1], 151552\n\t"
      "v_lshrrev_b32 v177, 2, %[l16]\n\t"
      "v_add_u32 v177, %[t1], v177\n\t"
      "ds_write_b128 v176, v[0:3]\n\t"
      "ds_write_b128 v176, v[4:7] offset:1024\n\t"
      "ds_write_b32 v177, v8\n\t"
      "s_add_u32 m0, %[l2], 81920\n\t"
      "s_nop 0\n\t"
      "global_load_lds_dwordx4 v172, s[46:47]\n\t"
      "s_add_u32 m0, %[l2], 82944\n\t"
      "s_nop 0\n\t"
      "global_load_lds_dwordx4 v173, s[46:47]\n\t"
      "s_add_u32 s46, s46, 0x8000\n\t"
      "s_addc_u32 s47, s47, 0\n\t"
      "v_smfmac_f32_32x32x32_bf16 v[16:31], v[144:147], v[88:95], %[idx]\n\t"
      "s_setprio 0\n\t"
      "v_xor_b32 v176, 0x2000, v176\n\t"
      "v_xor_b32 v177, 0x400, v177\n\t"
      "s_waitcnt vmcnt(4)\n\t"
      "s_waitcnt lgkmcnt(0)\n\t"
      "s_barrier\n\t"
      "ds_read_b128 v[0:3], v176\n\t"
      "ds_read_b128 v[4:7], v176 offset:1024\n\t"
      "ds_read_b32 v8, v177\n\t"
      "s_add_u32 m0, %[l2], 49152\n\t"
      "s_nop 0\n\t"
      "global_load_lds_dwordx4 v172, s[46:47]\n\t"
      "s_add_u32 m0, %[l2], 50176\n\t"
      "s_nop 0\n\t"
      "global_load_lds_dwordx4 v173, s[46:47]\n\t"
      "s_add_u32 s46, s46, 0x8000\n\t"
      "s_addc_u32 s47, s47, 0\n\t"
      "s_waitcnt lgkmcnt(0)\n\t"
      "v_pk_add_f32 v[16:17], v[16:17], v[0:1]\n\t"
      "v_pk_add_f32 v[18:19], v[18:19], v[2:3]\n\t"
      "v_pk_add_f32 v[20:21], v[20:21], v[4:5]\n\t"
      "v_pk_add_f32 v[22:23], v[22:23], v[6:7]\n\t"
      "v_add_f32 v24, v24, v8\n\t"
      : "=&{v[0:15]}"(o[0]), "=&{v[16:31]}"(o[1]), "+{v[96:111]}"(acc[0]), "+{v[112:127]}"(acc[1]), "=&{v[128:131]}"(qf[0]),
        "=&{v[132:135]}"(qf[1]), "=&{v[148:163]}"(hf), "=&{v[164:171]}"(wf), "=&{v[176:179]}"(ad), "+{v[184:185]}"(mm),
        "=&{v[192:207]}"(h2), "=&{v[212:227]}"(h3), "=&{v208}"(g), "+{s[44:47]}"(pp4), [t1] "=&s"(t1), [t2] "=&s"(t2), [t3] "=&s"(t3), [l2] "=&s"(s_l2), [l4] "=&s"(s_l4)
      : "{v[188:191]}"(da), [l16] "v"(l16), [idx] "v"(idx), [rs] "s"(rs), [ko] "s"(s_ko), [ho] "s"(s_ho), [l1] "s"(s_l1),
        [pw] "s"(s_pw), [po] "s"(s_po), [ga] "s"(s_ga), [gr] "s"(s_gr), [tw] "s"(s_tw)
      : "memory", "scc", "vcc", "v172", "v173", "v174", "v175", "v180", "v181", "v182", "v183", "v186", "v187", "s48", "s49",
        "v32", "v33", "v34", "v35", "v36", "v37", "v38", "v39", "v40", "v41", "v42", "v43", "v44", "v45", "v46", "v47", "v48", "v49", "v50", "v51", "v52", "v53", "v54", "v55", "v56", "v57", "v58", "v59", "v60", "v61", "v62", "v63", "v64", "v65", "v66", "v67", "v68", "v69", "v70", "v71", "v72", "v73", "v74", "v75", "v76", "v77", "v78", "v79", "v80", "v81", "v82", "v83", "v84", "v85", "v86", "v87", "v88", "v89", "v90", "v91", "v92", "v93", "v94", "v95", "v136", "v137", "v138", "v139", "v140", "v141", "v142", "v143", "v144", "v145", "v146", "v147"
  );
  f32x16 of = o[1];
  {
    float chk = of[8];
#pragma unroll
    for (int r = 0; r < 8; r++) chk = __builtin_fmaf(0.f, of[r], chk);
    const bool bad = !(chk <= 0x1p60f && chk >= 0x1p-60f);
    if (__builtin_expect(__any(bad), 0)) {
      const bf16x8 qq = qf[1];
      const unsigned kb = WS_KF + head * 65536, vb = WS_VF + head * 65536, vo = hi * 256 + (l31 & 15) * 16;
      float mt = -INFINITY;
#pragma unroll 1
      for (int t = 0; t < 64; t++) mt = __builtin_fmaxf(mt, max16(MFMA(bld(rs, l16, kb + t * 1024), qq, f32x16{})));
      mt = xmax(mt);
      of = f32x16{};
      const unsigned one2 = (lane & 11) == 0 ? 0x3F803F80u : 0u;
      const bf16x8 ones = __builtin_bit_cast(bf16x8, (u32x4){one2, one2, one2, one2});
#pragma unroll 1
      for (int t = 0; t < 64; t++) {
        f32x16 s = MFMA(bld(rs, l16, kb + t * 1024), qq, f32x16{});
        bf16x8 a0 = bld(rs, vo, vb + t * 1024), a1 = bld(rs, vo, vb + t * 1024 + 512);
        if (l31 >= 16) { a0 = ones; a1 = ones; }
#pragma unroll
        for (int r = 0; r < 16; r++) s[r] = __builtin_amdgcn_exp2f(s[r] - mt);
        of = SMFMAC(a0, lo8(s), of, idx);
        of = SMFMAC(a1, hi8(s), of, idx);
      }
    }
  }
  {
    u32x4 ov = pk8(of, 0, __builtin_amdgcn_rcpf(of[8]));
#pragma unroll
    for (int j = 0; j < 4; j++) ov[j] = min(ov[j], 0xFFFFFFFEu);
    __builtin_amdgcn_raw_buffer_store_b128(ov, rs, l16 + s_op, 0, 16);
  }
  u32x4 res;
  GAS u32x4* dst = (GAS u32x4*)((char*)out + (s_out + l31 * 1024 + hi * 16));
  asm volatile(
      "ds_read_b64_tr_b16 v[72:73], v178 offset:2048\n\t"
      "ds_read_b64_tr_b16 v[74:75], v178 offset:3072\n\t"
      "ds_read_b64_tr_b16 v[76:77], v179 offset:2048\n\t"
      "ds_read_b64_tr_b16 v[78:79], v179 offset:3072\n\t"
      "ds_read_b64_tr_b16 v[80:81], v178 offset:4096\n\t"
      "ds_read_b64_tr_b16 v[82:83], v178 offset:5120\n\t"
      "ds_read_b64_tr_b16 v[84:85], v179 offset:4096\n\t"
      "ds_read_b64_tr_b16 v[86:87], v179 offset:5120\n\t"
      "v_lshlrev_b32 v183, 6, %[l16]\n\t"
      "v_and_b32 v183, 0x1c00, v183\n\t"
      "s_add_u32 %[t2], %[os], 4096\n\t"
      "v_mfma_f32_32x32x16_bf16 v[96:111], v[164:167], v[212:215], v[96:111]\n\t"
      "ds_read_b64_tr_b16 v[88:89], v178 offset:6144\n\t"
      "ds_read_b64_tr_b16 v[90:91], v178 offset:7168\n\t"
      "ds_read_b64_tr_b16 v[92:93], v179 offset:6144\n\t"
      "ds_read_b64_tr_b16 v[94:95], v179 offset:7168\n\t"
      "v_mfma_f32_32x32x16_bf16 v[112:127], v[168:171], v[212:215], v[112:127]\n\t"
      "s_waitcnt lgkmcnt(8)\n\t"
      "v_mfma_f32_32x32x16_bf16 v[96:111], v[72:75], v[216:219], v[96:111]\n\t"
      "v_add_u32 v178, 4294868992, v178\n\t"
      "ds_read_b64_tr_b16 v[64:65], v178 offset:0\n\t"
      "ds_read_b64_tr_b16 v[66:67], v178 offset:1024\n\t"
      "v_add_u32 v179, 4294868992, v179\n\t"
      "ds_read_b64_tr_b16 v[68:69], v179 offset:0\n\t"
      "ds_read_b64_tr_b16 v[70:71], v179 offset:1024\n\t"
      "v_mfma_f32_32x32x16_bf16 v[112:127], v[76:79], v[216:219], v[112:127]\n\t"
      "s_waitcnt lgkmcnt(8)\n\t"
      "v_mfma_f32_32x32x16_bf16 v[96:111], v[80:83], v[220:223], v[96:111]\n\t"
      "ds_read_b64_tr_b16 v[72:73], v178 offset:2048\n\t"
      "ds_read_b64_tr_b16 v[74:75], v178 offset:3072\n\t"
      "ds_read_b64_tr_b16 v[76:77], v179 offset:2048\n\t"
      "ds_read_b64_tr_b16 v[78:79], v179 offset:3072\n\t"
      "v_mfma_f32_32x32x16_bf16 v[112:127], v[84:87], v[220:223], v[112:127]\n\t"
      "s_waitcnt lgkmcnt(8)\n\t"
      "v_mfma_f32_32x32x16_bf16 v[96:111], v[88:91], v[224:227], v[96:111]\n\t"
      "ds_read_b64_tr_b16 v[80:81], v178 offset:4096\n\t"
      "ds_read_b64_tr_b16 v[82:83], v178 offset:5120\n\t"
      "ds_read_b64_tr_b16 v[84:85], v179 offset:4096\n\t"
      "ds_read_b64_tr_b16 v[86:87], v179 offset:5120\n\t"
      "v_mfma_f32_32x32x16_bf16 v[112:127], v[92:95], v[224:227], v[112:127]\n\t"
      "s_waitcnt lgkmcnt(8)\n\t"
      "v_mfma_f32_32x32x16_bf16 v[96:111], v[64:67], v[192:195], v[96:111]\n\t"
      "ds_read_b64_tr_b16 v[88:89], v178 offset:6144\n\t"
      "ds_read_b64_tr_b16 v[90:91], v178 offset:7168\n\t"
      "ds_read_b64_tr_b16 v[92:93], v179 offset:6144\n\t"
      "ds_read_b64_tr_b16 v[94:95], v179 offset:7168\n\t"
      "v_mfma_f32_32x32x16_bf16 v[112:127], v[68:71], v[192:195], v[112:127]\n\t"
      "s_waitcnt lgkmcnt(8)\n\t"
      "v_mfma_f32_32x32x16_bf16 v[96:111], v[72:75], v[196:199], v[96:111]\n\t"
      "v_mfma_f32_32x32x16_bf16 v[112:127], v[76:79], v[196:199], v[112:127]\n\t"
      "s_waitcnt lgkmcnt(4)\n\t"
      "v_mfma_f32_32x32x16_bf16 v[96:111], v[80:83], v[200:203], v[96:111]\n\t"
      "v_mfma_f32_32x32x16_bf16 v[112:127], v[84:87], v[200:203], v[112:127]\n\t"
      "s_waitcnt lgkmcnt(0)\n\t"
      "v_mfma_f32_32x32x16_bf16 v[96:111], v[88:91], v[204:207], v[96:111]\n\t"
      "v_mfma_f32_32x32x16_bf16 v[112:127], v[92:95], v[204:207], v[112:127]\n\t"
      "buffer_load_dword v186, v183, %[rs], %[os] offen sc1\n\t"
      "s_waitcnt vmcnt(2)\n\t"
      "s_barrier\n\t"
      "s_lshl_b32 %[t1], %[kq], 12\n\t"
      "s_sub_u32 %[t1], 32768, %[t1]\n\t"
      "v_add_u32 v178, %[t1], v178\n\t"
      "v_add_u32 v179, %[t1], v179\n\t"
      "ds_read_b64_tr_b16 v[64:65], v178 offset:49152\n\t"
      "ds_read_b64_tr_b16 v[66:67], v178 offset:50176\n\t"
      "ds_read_b64_tr_b16 v[68:69], v179 offset:49152\n\t"
      "ds_read_b64_tr_b16 v[70:71], v179 offset:50176\n\t"
      "ds_read_b64_tr_b16 v[72:73], v178 offset:51200\n\t"
      "ds_read_b64_tr_b16 v[74:75], v178 offset:52224\n\t"
      "ds_read_b64_tr_b16 v[76:77], v179 offset:51200\n\t"
      "ds_read_b64_tr_b16 v[78:79], v179 offset:52224\n\t"
      "ds_read_b64_tr_b16 v[80:81], v178 offset:16384\n\t"
      "ds_read_b64_tr_b16 v[82:83], v178 offset:17408\n\t"
      "ds_read_b64_tr_b16 v[84:85], v179 offset:16384\n\t"
      "ds_read_b64_tr_b16 v[86:87], v179 offset:17408\n\t"
      "v_sub_f32 v187, 1.0, v208\n\t"
      "ds_read_b64_tr_b16 v[88:89], v178 offset:18432\n\t"
      "ds_read_b64_tr_b16 v[90:91], v178 offset:19456\n\t"
      "ds_read_b64_tr_b16 v[92:93], v179 offset:18432\n\t"
      "ds_read_b64_tr_b16 v[94:95], v179 offset:19456\n\t"
      "ds_read_b64_tr_b16 v[136:137], v178 offset:32768\n\t"
      "ds_read_b64_tr_b16 v[138:139], v178 offset:33792\n\t"
      "ds_read_b64_tr_b16 v[140:141], v179 offset:32768\n\t"
      "ds_read_b64_tr_b16 v[142:143], v179 offset:33792\n\t"
      "v_mul_f32 v96, v187, v96\n\t"
      "v_mul_f32 v97, v187, v97\n\t"
      "v_mul_f32 v98, v187, v98\n\t"
      "v_mul_f32 v99, v187, v99\n\t"
      "v_mul_f32 v100, v187, v100\n\t"
      "v_mul_f32 v101, v187, v101\n\t"
      "v_mul_f32 v102, v187, v102\n\t"
      "v_mul_f32 v103, v187, v103\n\t"
      "ds_read_b64_tr_b16 v[144:145], v178 offset:34816\n\t"
      "ds_read_b64_tr_b16 v[146:147], v178 offset:35840\n\t"
      "ds_read_b64_tr_b16 v[148:149], v179 offset:34816\n\t"
      "ds_read_b64_tr_b16 v[150:151], v179 offset:35840\n\t"
      "v_mul_f32 v104, v187, v104\n\t"
      "v_mul_f32 v105, v187, v105\n\t"
      "v_mul_f32 v106, v187, v106\n\t"
      "v_mul_f32 v107, v187, v107\n\t"
      "v_mul_f32 v108, v187, v108\n\t"
      "v_mul_f32 v109, v187, v109\n\t"
      "v_mul_f32 v110, v187, v110\n\t"
      "v_mul_f32 v111, v187, v111\n\t"
      "ds_read_b64_tr_b16 v[152:153], v178 offset:0\n\t"
      "ds_read_b64_tr_b16 v[154:155], v178 offset:1024\n\t"
      "ds_read_b64_tr_b16 v[156:157], v179 offset:0\n\t"
      "ds_read_b64_tr_b16 v[158:159], v179 offset:1024\n\t"
      "v_mul_f32 v112, v187, v112\n\t"
      "v_mul_f32 v113, v187, v113\n\t"
      "v_mul_f32 v114, v187, v114\n\t"
      "v_mul_f32 v115, v187, v115\n\t"
      "v_mul_f32 v116, v187, v116\n\t"
      "v_mul_f32 v117, v187, v117\n\t"
      "v_mul_f32 v118, v187, v118\n\t"
      "v_mul_f32 v119, v187, v119\n\t"
      "ds_read_b64_tr_b16 v[192:193], v178 offset:2048\n\t"
      "ds_read_b64_tr_b16 v[194:195], v178 offset:3072\n\t"
      "ds_read_b64_tr_b16 v[196:197], v179 offset:2048\n\t"
      "ds_read_b64_tr_b16 v[198:199], v179 offset:3072\n\t"
      "v_mul_f32 v120, v187, v120\n\t"
      "v_mul_f32 v121, v187, v121\n\t"
      "v_mul_f32 v122, v187, v122\n\t"
      "v_mul_f32 v123, v187, v123\n\t"
      "v_mul_f32 v124, v187, v124\n\t"
      "v_mul_f32 v125, v187, v125\n\t"
      "v_mul_f32 v126, v187, v126\n\t"
      "v_mul_f32 v127, v187, v127\n\t"
      "s_mov_b32 %[t1], 0x10000\n\t"
      "OC%=:\n\t"
      "s_waitcnt vmcnt(0)\n\t"
      "v_cmp_eq_u32 vcc, -1, v186\n\t"
      "s_cbranch_vccz OD%=\n\t"
      "s_sleep 2\n\t"
      "buffer_load_dword v186, v183, %[rs], %[os] offen sc1\n\t"
      "s_sub_u32 %[t1], %[t1], 1\n\t"
      "s_cmp_gt_i32 %[t1], 0\n\t"
      "s_cbranch_scc1 OC%=\n\t"
      "OD%=:\n\t"
      "buffer_load_dwordx4 v[0:3], %[l16], %[rs], %[os] offen offset:0 sc1\n\t"
      "buffer_load_dwordx4 v[4:7], %[l16], %[rs], %[os] offen offset:1024 sc1\n\t"
      "buffer_load_dwordx4 v[8:11], %[l16], %[rs], %[os] offen offset:2048 sc1\n\t"
      "buffer_load_dwordx4 v[12:15], %[l16], %[rs], %[os] offen offset:3072 sc1\n\t"
      "buffer_load_dwordx4 v[16:19], %[l16], %[rs], %[t2] offen offset:0 sc1\n\t"
      "buffer_load_dwordx4 v[20:23], %[l16], %[rs], %[t2] offen offset:1024 sc1\n\t"
      "buffer_load_dwordx4 v[24:27], %[l16], %[rs], %[t2] offen offset:2048 sc1\n\t"
      "buffer_load_dwordx4 v[28:31], %[l16], %[rs], %[t2] offen offset:3072 sc1\n\t"
      "s_branch OS%=\n\t"
      "OR%=:\n\t"
      "s_waitcnt vmcnt(0)\n\t"
      "s_sleep 3\n\t"
      "v_max3_u32 v187, v0, v1, v2\n\t"
      "v_max_u32 v187, v187, v3\n\t"
      "v_cmp_eq_u32 vcc, -1, v187\n\t"
      "s_cbranch_vccz ON0_%=\n\t"
      "buffer_load_dwordx4 v[0:3], %[l16], %[rs], %[os] offen offset:0 sc1\n\t"
      "ON0_%=:\n\t"
      "v_max3_u32 v187, v4, v5, v6\n\t"
      "v_max_u32 v187, v187, v7\n\t"
      "v_cmp_eq_u32 vcc, -1, v187\n\t"
      "s_cbranch_vccz ON1_%=\n\t"
      "buffer_load_dwordx4 v[4:7], %[l16], %[rs], %[os] offen offset:1024 sc1\n\t"
      "ON1_%=:\n\t"
      "v_max3_u32 v187, v8, v9, v10\n\t"
      "v_max_u32 v187, v187, v11\n\t"
      "v_cmp_eq_u32 vcc, -1, v187\n\t"
      "s_cbranch_vccz ON2_%=\n\t"
      "buffer_load_dwordx4 v[8:11], %[l16], %[rs], %[os] offen offset:2048 sc1\n\t"
      "ON2_%=:\n\t"
      "v_max3_u32 v187, v12, v13, v14\n\t"
      "v_max_u32 v187, v187, v15\n\t"
      "v_cmp_eq_u32 vcc, -1, v187\n\t"
      "s_cbranch_vccz ON3_%=\n\t"
      "buffer_load_dwordx4 v[12:15], %[l16], %[rs], %[os] offen offset:3072 sc1\n\t"
      "ON3_%=:\n\t"
      "v_max3_u32 v187, v16, v17, v18\n\t"
      "v_max_u32 v187, v187, v19\n\t"
      "v_cmp_eq_u32 vcc, -1, v187\n\t"
      "s_cbranch_vccz ON4_%=\n\t"
      "buffer_load_dwordx4 v[16:19], %[l16], %[rs], %[t2] offen offset:0 sc1\n\t"
      "ON4_%=:\n\t"
      "v_max3_u32 v187, v20, v21, v22\n\t"
      "v_max_u32 v187, v187, v23\n\t"
      "v_cmp_eq_u32 vcc, -1, v187\n\t"
      "s_cbranch_vccz ON5_%=\n\t"
      "buffer_load_dwordx4 v[20:23], %[l16], %[rs], %[t2] offen offset:1024 sc1\n\t"
      "ON5_%=:\n\t"
      "v_max3_u32 v187, v24, v25, v26\n\t"
      "v_max_u32 v187, v187, v27\n\t"
      "v_cmp_eq_u32 vcc, -1, v187\n\t"
      "s_cbranch_vccz ON6_%=\n\t"
      "buffer_load_dwordx4 v[24:27], %[l16], %[rs], %[t2] offen offset:2048 sc1\n\t"
      "ON6_%=:\n\t"
      "v_max3_u32 v187, v28, v29, v30\n\t"
      "v_max_u32 v187, v187, v31\n\t"
      "v_cmp_eq_u32 vcc, -1, v187\n\t"
      "s_cbranch_vccz ON7_%=\n\t"
      "buffer_load_dwordx4 v[28:31], %[l16], %[rs], %[t2] offen offset:3072 sc1\n\t"
      "ON7_%=:\n\t"
      "s_sub_u32 %[t1], %[t1], 1\n\t"
      "s_cmp_gt_i32 %[t1], 0\n\t"
      "s_cbranch_scc0 OX%=\n\t"
      "s_waitcnt vmcnt(0)\n\t"
      "s_setpc_b64 s[48:49]\n\t"
      "OS%=:\n\t"
      "s_waitcnt vmcnt(7) lgkmcnt(0)\n\t"
      "s_getpc_b64 s[48:49]\n\t"
      "v_max3_u32 v187, v0, v1, v2\n\t"
      "v_max_u32 v187, v187, v3\n\t"
      "v_cmp_eq_u32 vcc, -1, v187\n\t"
      "s_cbranch_vccnz OR%=\n\t"
      "v_mfma_f32_32x32x16_bf16 v[32:47], v[64:67], v[0:3], 0\n\t"
      "v_mfma_f32_32x32x16_bf16 v[48:63], v[68:71], v[0:3], 0\n\t"
      "s_waitcnt vmcnt(6)\n\t"
      "s_getpc_b64 s[48:49]\n\t"
      "v_max3_u32 v187, v4, v5, v6\n\t"
      "v_max_u32 v187, v187, v7\n\t"
      "v_cmp_eq_u32 vcc, -1, v187\n\t"
      "s_cbranch_vccnz OR%=\n\t"
      "v_mfma_f32_32x32x16_bf16 v[32:47], v[72:75], v[4:7], v[32:47]\n\t"
      "v_mfma_f32_32x32x16_bf16 v[48:63], v[76:79], v[4:7], v[48:63]\n\t"
      "s_waitcnt vmcnt(5)\n\t"
      "s_getpc_b64 s[48:49]\n\t"
      "v_max3_u32 v187, v8, v9, v10\n\t"
      "v_max_u32 v187, v187, v11\n\t"
      "v_cmp_eq_u32 vcc, -1, v187\n\t"
      "s_cbranch_vccnz OR%=\n\t"
      "v_mfma_f32_32x32x16_bf16 v[32:47], v[80:83], v[8:11], v[32:47]\n\t"
      "v_mfma_f32_32x32x16_bf16 v[48:63], v[84:87], v[8:11], v[48:63]\n\t"
      "s_waitcnt vmcnt(4)\n\t"
      "s_getpc_b64 s[48:49]\n\t"
      "v_max3_u32 v187, v12, v13, v14\n\t"
      "v_max_u32 v187, v187, v15\n\t"
      "v_cmp_eq_u32 vcc, -1, v187\n\t"
      "s_cbranch_vccnz OR%=\n\t"
      "v_mfma_f32_32x32x16_bf16 v[32:47], v[88:91], v[12:15], v[32:47]\n\t"
      "v_mfma_f32_32x32x16_bf16 v[48:63], v[92:95], v[12:15], v[48:63]\n\t"
      "s_waitcnt vmcnt(3)\n\t"
      "s_getpc_b64 s[48:49]\n\t"
      "v_max3_u32 v187, v16, v17, v18\n\t"
      "v_max_u32 v187, v187, v19\n\t"
      "v_cmp_eq_u32 vcc, -1, v187\n\t"
      "s_cbranch_vccnz OR%=\n\t"
      "v_mfma_f32_32x32x16_bf16 v[32:47], v[136:139], v[16:19], v[32:47]\n\t"
      "v_mfma_f32_32x32x16_bf16 v[48:63], v[140:143], v[16:19], v[48:63]\n\t"
      "s_waitcnt vmcnt(2)\n\t"
      "s_getpc_b64 s[48:49]\n\t"
      "v_max3_u32 v187, v20, v21, v22\n\t"
      "v_max_u32 v187, v187, v23\n\t"
      "v_cmp_eq_u32 vcc, -1, v187\n\t"
      "s_cbranch_vccnz OR%=\n\t"
      "v_mfma_f32_32x32x16_bf16 v[32:47], v[144:147], v[20:23], v[32:47]\n\t"
      "v_mfma_f32_32x32x16_bf16 v[48:63], v[148:151], v[20:23], v[48:63]\n\t"
      "s_waitcnt vmcnt(1)\n\t"
      "s_getpc_b64 s[48:49]\n\t"
      "v_max3_u32 v187, v24, v25, v26\n\t"
      "v_max_u32 v187, v187, v27\n\t"
      "v_cmp_eq_u32 vcc, -1, v187\n\t"
      "s_cbranch_vccnz OR%=\n\t"
      "v_mfma_f32_32x32x16_bf16 v[32:47], v[152:155], v[24:27], v[32:47]\n\t"
      "v_mfma_f32_32x32x16_bf16 v[48:63], v[156:159], v[24:27], v[48:63]\n\t"
      "s_waitcnt vmcnt(0)\n\t"
      "s_getpc_b64 s[48:49]\n\t"
      "v_max3_u32 v187, v28, v29, v30\n\t"
      "v_max_u32 v187, v187, v31\n\t"
      "v_cmp_eq_u32 vcc, -1, v187\n\t"
      "s_cbranch_vccnz OR%=\n\t"
      "v_mfma_f32_32x32x16_bf16 v[32:47], v[192:195], v[28:31], v[32:47]\n\t"
      "v_mfma_f32_32x32x16_bf16 v[48:63], v[196:199], v[28:31], v[48:63]\n\t"
      "OX%=:\n\t"
      "s_nop 10\n\t"
      "v_fmac_f32 v104, v208, v40\n\t"
      "v_fmac_f32 v105, v208, v41\n\t"
      "v_fmac_f32 v106, v208, v42\n\t"
      "v_fmac_f32 v107, v208, v43\n\t"
      "v_fmac_f32 v108, v208, v44\n\t"
      "v_fmac_f32 v109, v208, v45\n\t"
      "v_fmac_f32 v110, v208, v46\n\t"
      "v_fmac_f32 v111, v208, v47\n\t"
      "v_xor_b32 v187, 2048, %[rr]\n\t"
      "ds_write_b128 v187, v[104:107] offset:0\n\t"
      "ds_write_b128 v187, v[108:111] offset:1024\n\t"
      "v_fmac_f32 v96, v208, v32\n\t"
      "v_fmac_f32 v97, v208, v33\n\t"
      "v_fmac_f32 v98, v208, v34\n\t"
      "v_fmac_f32 v99, v208, v35\n\t"
      "v_fmac_f32 v100, v208, v36\n\t"
      "v_fmac_f32 v101, v208, v37\n\t"
      "v_fmac_f32 v102, v208, v38\n\t"
      "v_fmac_f32 v103, v208, v39\n\t"
      "v_fmac_f32 v112, v208, v48\n\t"
      "v_fmac_f32 v113, v208, v49\n\t"
      "v_fmac_f32 v114, v208, v50\n\t"
      "v_fmac_f32 v115, v208, v51\n\t"
      "v_fmac_f32 v116, v208, v52\n\t"
      "v_fmac_f32 v117, v208, v53\n\t"
      "v_fmac_f32 v118, v208, v54\n\t"
      "v_fmac_f32 v119, v208, v55\n\t"
      "v_xor_b32 v187, 4096, %[rr]\n\t"
      "ds_write_b128 v187, v[112:115] offset:8192\n\t"
      "ds_write_b128 v187, v[116:119] offset:9216\n\t"
      "v_fmac_f32 v120, v208, v56\n\t"
      "v_fmac_f32 v121, v208, v57\n\t"
      "v_fmac_f32 v122, v208, v58\n\t"
      "v_fmac_f32 v123, v208, v59\n\t"
      "v_fmac_f32 v124, v208, v60\n\t"
      "v_fmac_f32 v125, v208, v61\n\t"
      "v_fmac_f32 v126, v208, v62\n\t"
      "v_fmac_f32 v127, v208, v63\n\t"
      "v_xor_b32 v187, 6144, %[rr]\n\t"
      "ds_write_b128 v187, v[120:123] offset:16384\n\t"
      "ds_write_b128 v187, v[124:127] offset:17408\n\t"
      "s_waitcnt lgkmcnt(0)\n\t"
      "s_barrier\n\t"
      "ds_read_b128 v[0:3], %[rr] offset:0\n\t"
      "ds_read_b128 v[4:7], %[rr] offset:1024\n\t"
      "ds_read_b128 v[8:11], %[rr] offset:8192\n\t"
      "ds_read_b128 v[12:15], %[rr] offset:9216\n\t"
      "ds_read_b128 v[16:19], %[rr] offset:16384\n\t"
      "ds_read_b128 v[20:23], %[rr] offset:17408\n\t"
      "s_waitcnt lgkmcnt(4)\n\t"
      "v_pk_add_f32 v[0:1], v[0:1], v[96:97]\n\t"
      "v_pk_add_f32 v[2:3], v[2:3], v[98:99]\n\t"
      "v_pk_add_f32 v[4:5], v[4:5], v[100:101]\n\t"
      "v_pk_add_f32 v[6:7], v[6:7], v[102:103]\n\t"
      "s_waitcnt lgkmcnt(0)\n\t"
      "v_pk_add_f32 v[8:9], v[8:9], v[16:17]\n\t"
      "v_pk_add_f32 v[10:11], v[10:11], v[18:19]\n\t"
      "v_pk_add_f32 v[12:13], v[12:13], v[20:21]\n\t"
      "v_pk_add_f32 v[14:15], v[14:15], v[22:23]\n\t"
      "v_pk_add_f32 v[0:1], v[0:1], v[8:9]\n\t"
      "v_pk_add_f32 v[2:3], v[2:3], v[10:11]\n\t"
      "v_pk_add_f32 v[4:5], v[4:5], v[12:13]\n\t"
      "v_pk_add_f32 v[6:7], v[6:7], v[14:15]\n\t"
      "v_cvt_pk_bf16_f32 v0, v0, v1\n\t"
      "v_cvt_pk_bf16_f32 v1, v2, v3\n\t"
      "v_cvt_pk_bf16_f32 v2, v4, v5\n\t"
      "v_cvt_pk_bf16_f32 v3, v6, v7\n\t"
      : "=&{v[0:3]}"(res), "+{v[96:111]}"(acc[0]), "+{v[112:127]}"(acc[1]), "+{v[148:163]}"(hf), "+{v[176:179]}"(ad), "+{v[192:207]}"(h2),
        "+v"(dst), [t1] "=&s"(t1), [t2] "=&s"(t2)
      : "{v[164:171]}"(wf), "{v[212:227]}"(h3), "{v208}"(g), [l16] "v"(l16), [rr] "v"(lds0 + kh * 24576 + kq * 2048 + l16), [rs] "s"(rs), [kq] "s"(kq),
        [os] "s"(s_os)
      : "memory", "scc", "vcc", "v183", "v186", "v187", "s48", "s49",
        "v4", "v5", "v6", "v7", "v8", "v9", "v10", "v11", "v12", "v13", "v14", "v15", "v16", "v17", "v18", "v19", "v20", "v21", "v22", "v23", "v24", "v25", "v26", "v27", "v28", "v29", "v30", "v31", "v32", "v33", "v34", "v35", "v36", "v37", "v38", "v39", "v40", "v41", "v42", "v43", "v44", "v45", "v46", "v47", "v48", "v49", "v50", "v51", "v52", "v53", "v54", "v55", "v56", "v57", "v58", "v59", "v60", "v61", "v62", "v63", "v64", "v65", "v66", "v67", "v68", "v69", "v70", "v71", "v72", "v73", "v74", "v75", "v76", "v77", "v78", "v79", "v80", "v81", "v82", "v83", "v84", "v85", "v86", "v87", "v88", "v89", "v90", "v91", "v92", "v93", "v94", "v95", "v136", "v137", "v138", "v139", "v140", "v141", "v142", "v143", "v144", "v145", "v146", "v147"
  );
  *dst = res;
  asm volatile(".Lcode_a:");
}

extern "C" void kernel_launch(void* const* d_in, const int* in_sizes, int n_in, void* d_out, int out_size, void* d_ws,
                              size_t ws_size, hipStream_t stream) {
  (void)hipFuncSetAttribute((const void*)fused_kernel, hipFuncAttributeMaxDynamicSharedMemorySize, LDS_BYTES);
  fused_kernel<<<256, 512, LDS_BYTES, stream>>>((const u16*)d_in[0], (const u16*)d_in[1], (const u16*)d_in[2], (const u16*)d_in[4],
                                                (const u16*)d_in[5], (const u16*)d_in[6], (char*)d_ws, (u16*)d_out);
}
```
